```python
import torch

def _softmax(x, axis=-1):
    return torch.softmax(x, dim=axis)
def _gelu(x):
    return torch.nn.functional.gelu(x, approximate="tanh")
def _silu(x):
    return torch.nn.functional.silu(x)
def _sigmoid(x):
    return torch.sigmoid(x)
def _layernorm(x, axis=-1, eps=1e-5):
    m = x.mean(dim=axis, keepdim=True)
    v = x.var(dim=axis, keepdim=True, unbiased=False)
    return (x - m) / torch.sqrt(v + eps)
def _scatter_add(tgt, idx, src, axis=0):
    return tgt.clone().index_add_(axis, idx, src)
def _segment_sum(src, seg, n):
    out = torch.zeros((n,) + src.shape[1:], dtype=src.dtype)
    return out.index_add_(0, seg, src)
def _pad(x, pw):
    flat = tuple(v for pair in reversed(pw) for v in pair)
    return torch.nn.functional.pad(x, flat)

kernel_name = 'parallel_sum__mha_block__glu_gated_swiglu__prompt'

def setup_inputs():

    _w = (lambda a: (lambda t: t.to(torch.bfloat16) if t.is_floating_point() else t)(torch.as_tensor(a)))
    def _A_setup():
        B, S, D, H = 1, 2048, 512, 32
        return {
            "x": _w((lambda *s: torch.randn(*s, dtype=torch.bfloat16) * 0.05)(B, S, D)),
            "wqkv": _w((lambda *s: torch.randn(*s, dtype=torch.bfloat16) * 0.05)(D, 3 * D)),
            "wo": _w((lambda *s: torch.randn(*s, dtype=torch.bfloat16) * 0.05)(D, D)),
        }
    _A_in = _A_setup()

    def _B_setup():
        B, S, D, F = 1, 2048, 512, 1792
        return {
            "x": _w((lambda *s: torch.randn(*s, dtype=torch.bfloat16) * 0.05)(B, S, D)),
            "w1": _w((lambda *s: torch.randn(*s, dtype=torch.bfloat16) * 0.4)(D, F) * (2.0 / D) ** 0.5),
            "w3": _w((lambda *s: torch.randn(*s, dtype=torch.bfloat16) * 0.4)(D, F) * (2.0 / D) ** 0.5),
            "w2": _w((lambda *s: torch.randn(*s, dtype=torch.bfloat16) * 0.4)(F, D) * (2.0 / F) ** 0.5),
        }
    _B_in = _B_setup()

    return {
        "A_x": _A_in["x"],
        "A_wqkv": _A_in["wqkv"],
        "A_wo": _A_in["wo"],
        "B_x": _B_in["x"],
        "B_w1": _B_in["w1"],
        "B_w3": _B_in["w3"],
        "B_w2": _B_in["w2"],
    }

def reference(A_x, A_wqkv, A_wo, B_x, B_w1, B_w3, B_w2):
    def _A_ref(x, wqkv, wo):
        B, S, D = x.shape
        H = 32
        Dh = D // H
        qkv = (lambda x, *d: x.permute(*d))((x @ wqkv).reshape(B, S, 3, H, Dh), 2, 0, 3, 1, 4)
        q, k, v = qkv[0], qkv[1], qkv[2]
        scores = torch.einsum("bhqd,bhkd->bhqk", q, k) * (Dh ** -0.5)
        p = _softmax(scores, axis=-1)
        o = torch.einsum("bhqk,bhkd->bhqd", p, v)
        return (lambda x, *d: x.permute(*d))(o, 0, 2, 1, 3).reshape(B, S, D) @ wo

    def _B_ref(x, w1, w3, w2):
        gate = torch.einsum("bsd,df->bsf", x, w1)
        up = torch.einsum("bsd,df->bsf", x, w3)
        h = _silu(gate) * up
        out = torch.einsum("bsf,fd->bsd", h, w2)
        return out

    x = A_x
    xn = _layernorm(x, axis=-1)
    return x + _A_ref(xn, A_wqkv, A_wo) + _B_ref(xn, B_w1, B_w3, B_w2)
```

```cpp
#include <hip/hip_runtime.h>
#include <stdint.h>

typedef __bf16 bf16x2 __attribute__((ext_vector_type(2)));
typedef __bf16 bf16x4 __attribute__((ext_vector_type(4)));
typedef __bf16 bf16x8 __attribute__((ext_vector_type(8)));
typedef __bf16 bf16x16 __attribute__((ext_vector_type(16)));
typedef float f32x2 __attribute__((ext_vector_type(2)));
typedef float f32x4 __attribute__((ext_vector_type(4)));
typedef float f32x8 __attribute__((ext_vector_type(8)));
typedef float f32x16 __attribute__((ext_vector_type(16)));
typedef short s16x4 __attribute__((ext_vector_type(4)));
typedef unsigned u32x2 __attribute__((ext_vector_type(2)));
typedef unsigned u32x4 __attribute__((ext_vector_type(4)));
typedef unsigned short u16;

#define DEV __device__ __forceinline__
#define LDSP(T) __attribute__((address_space(3))) T*
#define WAIT_V(n) asm volatile("s_waitcnt vmcnt(" #n ")" ::: "memory")
#define WAIT_L(n) asm volatile("s_waitcnt lgkmcnt(" #n ")" ::: "memory")
#define BAR() do { asm volatile("s_waitcnt lgkmcnt(0)" ::: "memory"); __builtin_amdgcn_s_barrier(); asm volatile("" ::: "memory"); } while (0)

constexpr int S = 2048, D = 512, NH = 32, F = 1792;
constexpr size_t WS_XN = 0;
constexpr size_t WS_Q = WS_XN + 2u * S * D;
constexpr size_t WS_K = WS_Q + 2u * S * D;
constexpr size_t WS_V = WS_K + 2u * S * D;
constexpr size_t WS_O = WS_V + 2u * S * D;
constexpr size_t WS_H = WS_O + 2u * S * D;

DEV void glds16(const void* g, char* lds) {
  __builtin_amdgcn_global_load_lds((const __attribute__((address_space(1))) void*)g, (LDSP(void))lds, 16, 0, 0);
}
DEV bf16x8 lds_b128(const char* p) { return *(const LDSP(bf16x8))p; }
DEV bf16x8 lds_tr2(const char* p0, const char* p1) {
  s16x4 a = __builtin_amdgcn_ds_read_tr16_b64_v4i16((LDSP(s16x4))p0);
  s16x4 b = __builtin_amdgcn_ds_read_tr16_b64_v4i16((LDSP(s16x4))p1);
  return __builtin_bit_cast(bf16x8, __builtin_shufflevector(a, b, 0, 1, 2, 3, 4, 5, 6, 7));
}
DEV unsigned pk_bf16(float a, float b) {
  f32x2 v = {a, b};
  return __builtin_bit_cast(unsigned, __builtin_convertvector(v, bf16x2));
}
DEV float bf_lo(unsigned u) { return __builtin_bit_cast(float, u << 16); }
DEV float bf_hi(unsigned u) { return __builtin_bit_cast(float, u & 0xffff0000u); }
template <int CTRL> DEV float dpp_add(float v) {
  return v + __builtin_bit_cast(float, __builtin_amdgcn_update_dpp(0, __builtin_bit_cast(int, v), CTRL, 0xf, 0xf, true));
}
DEV float row16_sum(float v) {
  v = dpp_add<0xB1>(v);
  v = dpp_add<0x4E>(v);
  v = dpp_add<0x141>(v);
  v = dpp_add<0x140>(v);
  return v;
}
DEV f32x16 mfma32(bf16x8 a, bf16x8 b, f32x16 c) { return __builtin_amdgcn_mfma_f32_32x32x16_bf16(a, b, c, 0, 0, 0); }
DEV f32x4 mfma16(bf16x8 a, bf16x8 b, f32x4 c) { return __builtin_amdgcn_mfma_f32_16x16x32_bf16(a, b, c, 0, 0, 0); }

constexpr int K1_A = 65536;
constexpr int K1_K8 = 3104;
constexpr int K1_ST = 8 * K1_K8;
constexpr int K1_LDS = K1_A + 3 * K1_ST;

__global__ __launch_bounds__(512, 2) void k1_ln_qkv(const u16* __restrict__ x, const u16* __restrict__ wqkv, char* __restrict__ ws) {
  __shared__ __attribute__((aligned(16))) char smem[K1_LDS];
  const int tid = threadIdx.x, l = tid & 63;
  const int w = __builtin_amdgcn_readfirstlane(tid >> 6);
  const int b = blockIdx.x, nb = b & 7, mb = b >> 3;
  const int r4 = l >> 4, c = l & 15;
  u32x4 xr[2][4];
#pragma unroll
  for (int g = 0; g < 2; g++)
#pragma unroll
    for (int i = 0; i < 4; i++)
      xr[g][i] = *(const u32x4*)(x + (size_t)(mb * 64 + w * 8 + g * 4 + r4) * D + i * 128 + c * 8);
  const u16* wsrc = wqkv + (size_t)(w * 8 + ((l & 31) >> 2)) * (3 * D) + nb * 64 + (l >> 5) * 32 + (l & 3) * 8;
  char* bst = smem + K1_A + w * K1_K8;
#define K1_STAGE(s, buf)                                                       \
  do {                                                                         \
    const u16* p_ = wsrc + (size_t)(s) * 64 * (3 * D);                         \
    glds16(p_, bst + (buf) * K1_ST);                                           \
    glds16(p_ + D, bst + (buf) * K1_ST + 1024);                                \
    glds16(p_ + 2 * D, bst + (buf) * K1_ST + 2048);                            \
  } while (0)
  K1_STAGE(0, 0);
  K1_STAGE(1, 1);
#pragma unroll
  for (int g = 0; g < 2; g++) {
    float v[32];
    float s = 0.f;
#pragma unroll
    for (int i = 0; i < 4; i++)
#pragma unroll
      for (int j = 0; j < 4; j++) {
        v[i * 8 + 2 * j] = bf_lo(xr[g][i][j]);
        v[i * 8 + 2 * j + 1] = bf_hi(xr[g][i][j]);
      }
#pragma unroll
    for (int j = 0; j < 32; j++) s += v[j];
    const float m = row16_sum(s) * (1.0f / D);
    float q = 0.f;
#pragma unroll
    for (int j = 0; j < 32; j++) { v[j] -= m; q += v[j] * v[j]; }
    const float rs = __builtin_amdgcn_rsqf(row16_sum(q) * (1.0f / D) + 1e-5f);
    const int row = w * 8 + g * 4 + r4, tok = row & 31;
#pragma unroll
    for (int i = 0; i < 4; i++) {
      u32x4 o;
#pragma unroll
      for (int j = 0; j < 4; j++) o[j] = pk_bf16(v[i * 8 + 2 * j] * rs, v[i * 8 + 2 * j + 1] * rs);
      const int ks = 8 * i + (c >> 1), half = c & 1;
      *(LDSP(u32x4))(smem + (row >> 5) * 32768 + (ks * 64 + half * 32 + (tok ^ (c & 7))) * 16) = o;
      if (2 * i + (c >> 3) == nb)
        *(u32x4*)(ws + WS_XN + ((size_t)((mb * 2 + (row >> 5)) * 32 + ks) * 64 + half * 32 + tok) * 16) = o;
    }
  }
  const int mw = w & 1, nw = w >> 1, g = l >> 4;
  const char* ab[2];
#pragma unroll
  for (int p = 0; p < 2; p++)
    ab[p] = smem + mw * 32768 + (((g >> 1) * 64 + (g & 1) * 32) + ((l & 15) ^ (g | (4 * p)))) * 16;
  const char* bb = smem + K1_A + g * K1_K8 + (nw >> 1) * 512 + ((l & 15) >> 2) * 64 + (nw & 1) * 32 + (l & 3) * 8;
  f32x4 aq[2] = {}, ak[2] = {}, av[2] = {};
#pragma unroll
  for (int s = 0; s < 8; s++) {
    if (s < 7) WAIT_V(3); else WAIT_V(0);
    BAR();
    if (s + 2 < 8) K1_STAGE(s + 2, (s + 2) % 3);
#pragma unroll
    for (int p = 0; p < 2; p++) {
      const int ks32 = 2 * s + p;
      bf16x8 a0 = lds_b128(ab[p] + ks32 * 2048), a1 = lds_b128(ab[p] + ks32 * 2048 + 256);
      const char* bp = bb + (s % 3) * K1_ST + p * 4 * K1_K8;
      bf16x8 bq = lds_tr2(bp, bp + 256), bk = lds_tr2(bp + 1024, bp + 1280), bv = lds_tr2(bp + 2048, bp + 2304);
      aq[0] = mfma16(bq, a0, aq[0]); aq[1] = mfma16(bq, a1, aq[1]);
      ak[0] = mfma16(bk, a0, ak[0]); ak[1] = mfma16(bk, a1, ak[1]);
      av[0] = mfma16(a0, bv, av[0]); av[1] = mfma16(a1, bv, av[1]);
    }
  }
  const int head = nb * 4 + nw, tile = mb * 2 + mw;
  const size_t tb = ((size_t)head * 64 + tile) * 1024;
  const float qs = 0.25f * 1.4426950408889634f;
#pragma unroll
  for (int a = 0; a < 2; a++) {
    const size_t o = tb + ((g >> 1) * 32 + a * 16 + (l & 15)) * 16 + (g & 1) * 8;
    *(u32x2*)(ws + WS_Q + o) = u32x2{pk_bf16(aq[a][0] * qs, aq[a][1] * qs), pk_bf16(aq[a][2] * qs, aq[a][3] * qs)};
    *(u32x2*)(ws + WS_K + o) = u32x2{pk_bf16(ak[a][0], ak[a][1]), pk_bf16(ak[a][2], ak[a][3])};
    const size_t ov = tb + a * 512 + ((g >> 1) * 16 + (l & 15)) * 16 + (g & 1) * 8;
    *(u32x2*)(ws + WS_V + ov) = u32x2{pk_bf16(av[a][0], av[a][1]), pk_bf16(av[a][2], av[a][3])};
  }
#undef K1_STAGE
}

constexpr int AT_CH = 8 * 1024 + 8 * 1152;
constexpr int AT_LDS = 3 * AT_CH;

DEV f32x16 pv_step(bf16x8 v, f32x8 p, f32x16 acc) {
  return __builtin_amdgcn_smfmac_f32_32x32x32_bf16(v, __builtin_bit_cast(bf16x16, p), acc, (int)0xDDDDDDDD, 0, 0);
}

DEV void attn_body(char* smem, const char* ws_c, char* ws, int head, int qb) {
  const int tid = threadIdx.x, l = tid & 63;
  const int w = __builtin_amdgcn_readfirstlane(tid >> 6);
  const char* kg = ws_c + WS_K + (size_t)head * 65536 + l * 16;
  const char* vg = ws_c + WS_V + (size_t)head * 65536 + l * 16;
  const int qt = qb * 8 + w;
  const bf16x8 qf = *(const bf16x8*)(ws_c + WS_Q + ((size_t)head * 64 + qt) * 1024 + l * 16);
  if (tid < 3 * 8 * 8) {
    const int t = tid >> 3;
    *(LDSP(u32x4))(smem + (t >> 3) * AT_CH + 8192 + (t & 7) * 1152 + 1024 + (tid & 7) * 16) =
        u32x4{0x3F803F80u, 0x3F803F80u, 0x3F803F80u, 0x3F803F80u};
  }
#define AT_STAGE(ch, buf)                                                      \
  do {                                                                         \
    glds16(kg + ((ch) * 8 + w) * 1024, smem + (buf) * AT_CH + w * 1024);       \
    glds16(vg + ((ch) * 8 + w) * 1024, smem + (buf) * AT_CH + 8192 + w * 1152);\
  } while (0)
  AT_STAGE(0, 0);
  AT_STAGE(1, 1);
  const int d = l & 31, ha = l >> 5;
  const int cst = 8192 + 1024 + ((d >= 20 && d < 28) ? 64 : 0);
  const int va0 = d < 16 ? 8192 + (ha * 16 + d) * 16 : cst;
  const int va1 = d < 16 ? va0 + 512 : cst;
  f32x16 acc = {};
  int buf = 0;
  for (int ch = 0; ch < 8; ch++) {
    if (ch < 7) WAIT_V(2); else WAIT_V(0);
    BAR();
    if (ch + 2 < 8) { const int nb = buf >= 1 ? buf - 1 : 2; AT_STAGE(ch + 2, nb); }
    const char* cb = smem + buf * AT_CH;
    const char* kp = cb + l * 16;
    const char* v0 = cb + va0;
    const char* v1 = cb + va1;
#pragma unroll
    for (int t = 0; t < 8; t++) {
      const bf16x8 kf = lds_b128(kp + t * 1024);
      const bf16x8 x0 = lds_b128(v0 + t * 1152), x1 = lds_b128(v1 + t * 1152);
      f32x16 s = mfma32(kf, qf, f32x16{});
      f32x8 p0, p1;
#pragma unroll
      for (int j = 0; j < 8; j++) { p0[j] = __builtin_amdgcn_exp2f(s[j]); p1[j] = __builtin_amdgcn_exp2f(s[8 + j]); }
      acc = pv_step(x0, p0, acc);
      acc = pv_step(x1, p1, acc);
    }
    buf = buf == 2 ? 0 : buf + 1;
  }
#undef AT_STAGE
  float lsum = acc[8];
  bool ok = lsum >= 0x1p-40f && lsum <= 0x1p40f;
#pragma unroll
  for (int j = 0; j < 8; j++) ok = ok && (__builtin_fabsf(acc[j]) < 0x1p120f);
  if (__builtin_amdgcn_ballot_w64(!ok) != 0) {
    float m = -__builtin_inff();
    acc = f32x16{};
    const bf16x8 ones = __builtin_bit_cast(bf16x8, u32x4{0x3F803F80u, 0x3F803F80u, 0x3F803F80u, 0x3F803F80u});
    const char* vl = ws_c + WS_V + (size_t)head * 65536 + (ha * 16 + (d & 15)) * 16;
    for (int t = 0; t < 64; t++) {
      const bf16x8 kf = *(const bf16x8*)(kg + t * 1024);
      bf16x8 x0 = *(const bf16x8*)(vl + t * 1024), x1 = *(const bf16x8*)(vl + t * 1024 + 512);
      if (d >= 16) { x0 = ones; x1 = ones; }
      f32x16 s = mfma32(kf, qf, f32x16{});
      float tm = s[0];
#pragma unroll
      for (int j = 1; j < 16; j++) tm = __builtin_fmaxf(tm, s[j]);
      tm = __builtin_fmaxf(tm, __shfl_xor(tm, 32));
      const float mn = __builtin_fmaxf(m, tm);
      const float al = __builtin_amdgcn_exp2f(m - mn);
      m = mn;
      f32x8 p0, p1;
#pragma unroll
      for (int j = 0; j < 8; j++) { p0[j] = __builtin_amdgcn_exp2f(s[j] - mn); p1[j] = __builtin_amdgcn_exp2f(s[8 + j] - mn); }
#pragma unroll
      for (int j = 0; j < 16; j++) acc[j] *= al;
      acc = pv_step(x0, p0, acc);
      acc = pv_step(x1, p1, acc);
    }
    lsum = acc[8];
  }
  const float inv = 1.0f / lsum;
  u32x4 o;
#pragma unroll
  for (int j = 0; j < 4; j++) o[j] = pk_bf16(acc[2 * j] * inv, acc[2 * j + 1] * inv);
  *(u32x4*)(ws + WS_O + ((size_t)qt * 32 + head) * 1024 + l * 16) = o;
}

__global__ __launch_bounds__(512, 2) void k2_attn(char* __restrict__ ws) {
  __shared__ __attribute__((aligned(16))) char smem[AT_LDS];
  const int b = blockIdx.x;
  attn_body(smem, ws, ws, (b & 7) * 4 + ((b >> 3) & 3), b >> 5);
}

constexpr int GU_ST = 8192 + 16384;
constexpr int GU_LDS = 3 * GU_ST;

DEV void gateup_body(char* smem, const u16* __restrict__ w1, const u16* __restrict__ w3, char* ws, int mb, int nb) {
  const int tid = threadIdx.x, l = tid & 63;
  const int w = __builtin_amdgcn_readfirstlane(tid >> 6);
  const char* ag = ws + WS_XN + ((size_t)(mb * 4 + (w >> 1)) * 32 + (w & 1)) * 1024 + l * 16;
  const u16* bg = ((w & 1) ? w3 : w1) + (size_t)((w >> 1) * 8 + ((l & 31) >> 2)) * F + nb * 128 + (l >> 5) * 32 + (l & 3) * 8;
#define GU_STAGE(s, buf)                                                       \
  do {                                                                         \
    glds16(ag + (s) * 2048, smem + (buf) * GU_ST + w * 1024);                  \
    const u16* p_ = bg + (size_t)(s) * 32 * F;                                 \
    glds16(p_, smem + (buf) * GU_ST + 8192 + w * 2048);                        \
    glds16(p_ + 64, smem + (buf) * GU_ST + 8192 + w * 2048 + 1024);            \
  } while (0)
  GU_STAGE(0, 0);
  GU_STAGE(1, 1);
  const int mw = w & 1, nw = w >> 1, h = l >> 5;
  const int aoff = mw * 4096 + l * 16;
  const int boff = 8192 + h * 4096 + nw * 512 + ((l & 15) >> 2) * 64 + ((l >> 4) & 1) * 32 + (l & 3) * 8;
  f32x16 ga[2] = {}, ua[2] = {};
  int buf = 0;
  for (int s = 0; s < 16; s++) {
    if (s < 15) WAIT_V(3); else WAIT_V(0);
    BAR();
    if (s + 2 < 16) { const int nbuf = buf >= 1 ? buf - 1 : 2; GU_STAGE(s + 2, nbuf); }
    const char* sb = smem + buf * GU_ST;
#pragma unroll
    for (int ks = 0; ks < 2; ks++) {
      const bf16x8 a0 = lds_b128(sb + aoff + ks * 1024), a1 = lds_b128(sb + aoff + 2048 + ks * 1024);
      const char* bp = sb + boff + ks * 8192;
      const bf16x8 bg_ = lds_tr2(bp, bp + 256), bu_ = lds_tr2(bp + 2048, bp + 2304);
      ga[0] = mfma32(bg_, a0, ga[0]); ua[0] = mfma32(bu_, a0, ua[0]);
      ga[1] = mfma32(bg_, a1, ga[1]); ua[1] = mfma32(bu_, a1, ua[1]);
    }
    buf = buf == 2 ? 0 : buf + 1;
  }
#undef GU_STAGE
#pragma unroll
  for (int a = 0; a < 2; a++) {
    const int mblk = mb * 4 + mw * 2 + a;
#pragma unroll
    for (int hs = 0; hs < 2; hs++) {
      u32x4 o;
#pragma unroll
      for (int j = 0; j < 4; j++) {
        float r[2];
#pragma unroll
        for (int e = 0; e < 2; e++) {
          const float gv = ga[a][hs * 8 + 2 * j + e], uv = ua[a][hs * 8 + 2 * j + e];
          const float sg = __builtin_amdgcn_rcpf(1.0f + __builtin_amdgcn_exp2f(gv * -1.4426950408889634f));
          r[e] = gv * sg * uv;
        }
        o[j] = pk_bf16(r[0], r[1]);
      }
      const int ks = nb * 8 + nw * 2 + hs;
      *(u32x4*)(ws + WS_H + ((size_t)mblk * 112 + ks) * 1024 + l * 16) = o;
    }
  }
}

__global__ __launch_bounds__(512, 2) void k2_gateup(const u16* __restrict__ w1, const u16* __restrict__ w3, char* __restrict__ ws) {
  __shared__ __attribute__((aligned(16))) char smem[GU_LDS];
  const int b = blockIdx.x;
  gateup_body(smem, w1, w3, ws, b / 14, b % 14);
}

constexpr int FN_ST = 32768;
constexpr int FN_LDS = 3 * FN_ST;

__global__ __launch_bounds__(512, 2) void k3_final(const u16* __restrict__ x, const u16* __restrict__ w2, const u16* __restrict__ wo,
                                                   const char* __restrict__ ws, u16* __restrict__ out) {
  __shared__ __attribute__((aligned(16))) char smem[FN_LDS];
  const int tid = threadIdx.x, l = tid & 63;
  const int w = __builtin_amdgcn_readfirstlane(tid >> 6);
  const int b = blockIdx.x, nb = b & 7, mb = b >> 3;
  const char* ah = ws + WS_H + ((size_t)(mb * 2 + (w >> 2)) * 112 + (w & 3) * 2) * 1024 + l * 16;
  const char* ao = ws + WS_O + ((size_t)(mb * 2 + (w >> 2)) * 32 + (w & 3) * 2) * 1024 + l * 16;
  const size_t brow = (size_t)(w * 16 + ((l & 31) >> 2)) * D + nb * 64 + (l >> 5) * 32 + (l & 3) * 8;
#define FN_STAGE(s, buf)                                                       \
  do {                                                                         \
    const char* a_ = (s) < 14 ? ah + (s) * 8192 : ao + ((s) - 14) * 8192;      \
    const u16* p_ = ((s) < 14 ? w2 + (size_t)(s) * 128 * D : wo + (size_t)((s) - 14) * 128 * D) + brow; \
    char* d_ = smem + (buf) * FN_ST;                                           \
    glds16(a_, d_ + (w >> 2) * 8192 + (w & 3) * 2048);                         \
    glds16(a_ + 1024, d_ + (w >> 2) * 8192 + (w & 3) * 2048 + 1024);           \
    glds16(p_, d_ + 16384 + w * 2048);                                         \
    glds16(p_ + 8 * D, d_ + 16384 + w * 2048 + 1024);                          \
  } while (0)
  FN_STAGE(0, 0);
  FN_STAGE(1, 1);
  const int nh = w & 1, kq = w >> 1, h = l >> 5;
  const int aoff = kq * 2048 + l * 16;
  const int boff = 16384 + kq * 4096 + nh * 512 + (4 * h + ((l & 15) >> 2)) * 64 + ((l >> 4) & 1) * 32 + (l & 3) * 8;
  f32x16 acc[2] = {};
  int buf = 0;
  for (int s = 0; s < 18; s++) {
    if (s < 17) WAIT_V(4); else WAIT_V(0);
    BAR();
    if (s + 2 < 18) { const int nbuf = buf >= 1 ? buf - 1 : 2; FN_STAGE(s + 2, nbuf); }
    const char* sb = smem + buf * FN_ST;
#pragma unroll
    for (int ks = 0; ks < 2; ks++) {
      const bf16x8 a0 = lds_b128(sb + aoff + ks * 1024), a1 = lds_b128(sb + aoff + 8192 + ks * 1024);
      const char* bp = sb + boff + ks * 2048;
      const bf16x8 bw = lds_tr2(bp, bp + 1024);
      acc[0] = mfma32(bw, a0, acc[0]);
      acc[1] = mfma32(bw, a1, acc[1]);
    }
    buf = buf == 2 ? 0 : buf + 1;
  }
#undef FN_STAGE
  BAR();
#pragma unroll
  for (int a = 0; a < 2; a++)
#pragma unroll
    for (int j = 0; j < 4; j++) {
      f32x4 v = {acc[a][4 * j], acc[a][4 * j + 1], acc[a][4 * j + 2], acc[a][4 * j + 3]};
      *(LDSP(f32x4))(smem + ((kq * 64 + a * 32 + (l & 31)) * 68 + nh * 32 + 8 * j + 4 * h) * 4) = v;
    }
  BAR();
  const int tok = tid >> 3, n8 = (tid & 7) * 8;
  const size_t go = (size_t)(mb * 64 + tok) * D + nb * 64 + n8;
  const u32x4 xv = *(const u32x4*)(x + go);
  f32x4 s0 = {}, s1 = {};
#pragma unroll
  for (int q = 0; q < 4; q++) {
    const char* p = smem + ((q * 64 + tok) * 68 + n8) * 4;
    s0 += *(const LDSP(f32x4))p;
    s1 += *(const LDSP(f32x4))(p + 16);
  }
  u32x4 o;
  o[0] = pk_bf16(s0[0] + bf_lo(xv[0]), s0[1] + bf_hi(xv[0]));
  o[1] = pk_bf16(s0[2] + bf_lo(xv[1]), s0[3] + bf_hi(xv[1]));
  o[2] = pk_bf16(s1[0] + bf_lo(xv[2]), s1[1] + bf_hi(xv[2]));
  o[3] = pk_bf16(s1[2] + bf_lo(xv[3]), s1[3] + bf_hi(xv[3]));
  *(u32x4*)(out + go) = o;
}

extern "C" void kernel_launch(void* const* d_in, const int* in_sizes, int n_in, void* d_out, int out_size, void* d_ws,
                              size_t ws_size, hipStream_t stream) {
  const u16* x = (const u16*)d_in[0];
  const u16* wqkv = (const u16*)d_in[1];
  const u16* wo = (const u16*)d_in[2];
  const u16* w1 = (const u16*)d_in[4];
  const u16* w3 = (const u16*)d_in[5];
  const u16* w2 = (const u16*)d_in[6];
  char* ws = (char*)d_ws;
  k1_ln_qkv<<<256, 512, 0, stream>>>(x, wqkv, ws);
  k2_attn<<<256, 512, 0, stream>>>(ws);
  k2_gateup<<<224, 512, 0, stream>>>(w1, w3, ws);
  k3_final<<<256, 512, 0, stream>>>(x, w2, wo, ws, (u16*)d_out);
}
```

```cpp
#include <hip/hip_runtime.h>
#include <stdint.h>

typedef __bf16 bf16x2 __attribute__((ext_vector_type(2)));
typedef __bf16 bf16x4 __attribute__((ext_vector_type(4)));
typedef __bf16 bf16x8 __attribute__((ext_vector_type(8)));
typedef __bf16 bf16x16 __attribute__((ext_vector_type(16)));
typedef float f32x2 __attribute__((ext_vector_type(2)));
typedef float f32x4 __attribute__((ext_vector_type(4)));
typedef float f32x8 __attribute__((ext_vector_type(8)));
typedef float f32x16 __attribute__((ext_vector_type(16)));
typedef short s16x4 __attribute__((ext_vector_type(4)));
typedef unsigned u32x2 __attribute__((ext_vector_type(2)));
typedef unsigned u32x4 __attribute__((ext_vector_type(4)));
typedef unsigned short u16;

#define DEV __device__ __forceinline__
#define LDSP(T) __attribute__((address_space(3))) T*
#define WAIT_V(n) asm volatile("s_waitcnt vmcnt(" #n ")" ::: "memory")
#define WAIT_L(n) asm volatile("s_waitcnt lgkmcnt(" #n ")" ::: "memory")
#define BAR() do { asm volatile("s_waitcnt lgkmcnt(0)" ::: "memory"); __builtin_amdgcn_s_barrier(); asm volatile("" ::: "memory"); } while (0)

constexpr int S = 2048, D = 512, NH = 32, F = 1792;
constexpr size_t WS_XN = 0;
constexpr size_t WS_Q = WS_XN + 2u * S * D;
constexpr size_t WS_K = WS_Q + 2u * S * D;
constexpr size_t WS_V = WS_K + 2u * S * D;
constexpr size_t WS_O = WS_V + 2u * S * D;
constexpr size_t WS_H = WS_O + 2u * S * D;

DEV void glds16(const void* base, unsigned voff, unsigned lds) {
  unsigned keep;
  asm volatile("s_mov_b32 %0, m0\n\ts_mov_b32 m0, %3\n\ts_nop 0\n\tglobal_load_lds_dwordx4 %1, %2\n\ts_mov_b32 m0, %0"
               : "=&s"(keep) : "v"(voff), "s"(base), "s"(lds) : "memory");
}
DEV unsigned lds_addr(const char* p) { return (unsigned)(uintptr_t)(const LDSP(char))p; }
DEV bf16x8 lds_b128(const char* p) { return *(const LDSP(bf16x8))p; }
DEV bf16x8 lds_tr2(const char* p0, const char* p1) {
  s16x4 a = __builtin_amdgcn_ds_read_tr16_b64_v4i16((LDSP(s16x4))p0);
  s16x4 b = __builtin_amdgcn_ds_read_tr16_b64_v4i16((LDSP(s16x4))p1);
  return __builtin_bit_cast(bf16x8, __builtin_shufflevector(a, b, 0, 1, 2, 3, 4, 5, 6, 7));
}
DEV unsigned pk_bf16(float a, float b) {
  f32x2 v = {a, b};
  return __builtin_bit_cast(unsigned, __builtin_convertvector(v, bf16x2));
}
DEV float bf_lo(unsigned u) { return __builtin_bit_cast(float, u << 16); }
DEV float bf_hi(unsigned u) { return __builtin_bit_cast(float, u & 0xffff0000u); }
template <int CTRL> DEV float dpp_add(float v) {
  return v + __builtin_bit_cast(float, __builtin_amdgcn_update_dpp(0, __builtin_bit_cast(int, v), CTRL, 0xf, 0xf, true));
}
DEV float row16_sum(float v) {
  v = dpp_add<0xB1>(v);
  v = dpp_add<0x4E>(v);
  v = dpp_add<0x141>(v);
  v = dpp_add<0x140>(v);
  return v;
}
DEV f32x16 mfma32(bf16x8 a, bf16x8 b, f32x16 c) { return __builtin_amdgcn_mfma_f32_32x32x16_bf16(a, b, c, 0, 0, 0); }
DEV f32x4 mfma16(bf16x8 a, bf16x8 b, f32x4 c) { return __builtin_amdgcn_mfma_f32_16x16x32_bf16(a, b, c, 0, 0, 0); }

constexpr int K1_A = 65536;
constexpr int K1_K8 = 3104;
constexpr int K1_ST = 8 * K1_K8;
constexpr int K1_LDS = K1_A + 3 * K1_ST;

__global__ __launch_bounds__(512, 2) void k1_ln_qkv(const u16* __restrict__ x, const u16* __restrict__ wqkv, char* __restrict__ ws) {
  __shared__ __attribute__((aligned(16))) char smem[K1_LDS];
  const int tid = threadIdx.x, l = tid & 63;
  const int w = __builtin_amdgcn_readfirstlane(tid >> 6);
  const int b = blockIdx.x, nb = b & 7, mb = b >> 3;
  const int r4 = l >> 4, c = l & 15;
  u32x4 xr[2][4];
#pragma unroll
  for (int g = 0; g < 2; g++)
#pragma unroll
    for (int i = 0; i < 4; i++)
      xr[g][i] = *(const u32x4*)(x + (size_t)(mb * 64 + w * 8 + g * 4 + r4) * D + i * 128 + c * 8);
  const u16* wsrc = wqkv + (size_t)(w * 8) * (3 * D) + nb * 64;
  const unsigned wvo = (((l & 31) >> 2) * (3 * D) + (l >> 5) * 32 + (l & 3) * 8) * 2;
  const unsigned bst = lds_addr(smem) + K1_A + w * K1_K8;
#define K1_STAGE(s, buf)                                                       \
  do {                                                                         \
    const u16* p_ = wsrc + (size_t)(s) * 64 * (3 * D);                         \
    glds16(p_, wvo, bst + (buf) * K1_ST);                                      \
    glds16(p_ + D, wvo, bst + (buf) * K1_ST + 1024);                           \
    glds16(p_ + 2 * D, wvo, bst + (buf) * K1_ST + 2048);                       \
  } while (0)
  K1_STAGE(0, 0);
  K1_STAGE(1, 1);
#pragma unroll
  for (int g = 0; g < 2; g++) {
    float v[32];
    float s = 0.f;
#pragma unroll
    for (int i = 0; i < 4; i++)
#pragma unroll
      for (int j = 0; j < 4; j++) {
        v[i * 8 + 2 * j] = bf_lo(xr[g][i][j]);
        v[i * 8 + 2 * j + 1] = bf_hi(xr[g][i][j]);
      }
#pragma unroll
    for (int j = 0; j < 32; j++) s += v[j];
    const float m = row16_sum(s) * (1.0f / D);
    float q = 0.f;
#pragma unroll
    for (int j = 0; j < 32; j++) { v[j] -= m; q += v[j] * v[j]; }
    const float rs = __builtin_amdgcn_rsqf(row16_sum(q) * (1.0f / D) + 1e-5f);
    const int row = w * 8 + g * 4 + r4, tok = row & 31;
#pragma unroll
    for (int i = 0; i < 4; i++) {
      u32x4 o;
#pragma unroll
      for (int j = 0; j < 4; j++) o[j] = pk_bf16(v[i * 8 + 2 * j] * rs, v[i * 8 + 2 * j + 1] * rs);
      const int ks = 8 * i + (c >> 1), half = c & 1;
      *(LDSP(u32x4))(smem + (row >> 5) * 32768 + (ks * 64 + half * 32 + (tok ^ (c & 7))) * 16) = o;
      if (2 * i + (c >> 3) == nb)
        *(u32x4*)(ws + WS_XN + ((size_t)((mb * 2 + (row >> 5)) * 32 + ks) * 64 + half * 32 + tok) * 16) = o;
    }
  }
  const int mw = w & 1, nw = w >> 1, g = l >> 4;
  const char* ab[2];
#pragma unroll
  for (int p = 0; p < 2; p++)
    ab[p] = smem + mw * 32768 + (((g >> 1) * 64 + (g & 1) * 32) + ((l & 15) ^ (g | (4 * p)))) * 16;
  const char* bb = smem + K1_A + g * K1_K8 + (nw >> 1) * 512 + ((l & 15) >> 2) * 64 + (nw & 1) * 32 + (l & 3) * 8;
  f32x4 aq[2] = {}, ak[2] = {}, av[2] = {};
#pragma unroll
  for (int s = 0; s < 8; s++) {
    if (s < 7) WAIT_V(3); else WAIT_V(0);
    BAR();
    if (s + 2 < 8) K1_STAGE(s + 2, (s + 2) % 3);
#pragma unroll
    for (int p = 0; p < 2; p++) {
      const int ks32 = 2 * s + p;
      bf16x8 a0 = lds_b128(ab[p] + ks32 * 2048), a1 = lds_b128(ab[p] + ks32 * 2048 + 256);
      const char* bp = bb + (s % 3) * K1_ST + p * 4 * K1_K8;
      bf16x8 bq = lds_tr2(bp, bp + 256), bk = lds_tr2(bp + 1024, bp + 1280), bv = lds_tr2(bp + 2048, bp + 2304);
      aq[0] = mfma16(bq, a0, aq[0]); aq[1] = mfma16(bq, a1, aq[1]);
      ak[0] = mfma16(bk, a0, ak[0]); ak[1] = mfma16(bk, a1, ak[1]);
      av[0] = mfma16(a0, bv, av[0]); av[1] = mfma16(a1, bv, av[1]);
    }
  }
  const int head = nb * 4 + nw, tile = mb * 2 + mw;
  const size_t tb = ((size_t)head * 64 + tile) * 1024;
  const float qs = 0.25f * 1.4426950408889634f;
#pragma unroll
  for (int a = 0; a < 2; a++) {
    const size_t o = tb + ((g >> 1) * 32 + a * 16 + (l & 15)) * 16 + (g & 1) * 8;
    *(u32x2*)(ws + WS_Q + o) = u32x2{pk_bf16(aq[a][0] * qs, aq[a][1] * qs), pk_bf16(aq[a][2] * qs, aq[a][3] * qs)};
    *(u32x2*)(ws + WS_K + o) = u32x2{pk_bf16(ak[a][0], ak[a][1]), pk_bf16(ak[a][2], ak[a][3])};
    const size_t ov = tb + a * 512 + ((g >> 1) * 16 + (l & 15)) * 16 + (g & 1) * 8;
    *(u32x2*)(ws + WS_V + ov) = u32x2{pk_bf16(av[a][0], av[a][1]), pk_bf16(av[a][2], av[a][3])};
  }
#undef K1_STAGE
}

constexpr int AT_CH = 8 * 1024 + 8 * 1152;
constexpr int AT_LDS = 3 * AT_CH;

DEV f32x16 pv_step(bf16x8 v, f32x8 p, f32x16 acc) {
  return __builtin_amdgcn_smfmac_f32_32x32x32_bf16(v, __builtin_bit_cast(bf16x16, p), acc, (int)0xDDDDDDDD, 0, 0);
}

DEV void attn_body(char* smem, const char* ws_c, char* ws, int head, int qb) {
  const int tid = threadIdx.x, l = tid & 63;
  const int w = __builtin_amdgcn_readfirstlane(tid >> 6);
  const char* kg = ws_c + WS_K + (size_t)head * 65536;
  const char* vg = ws_c + WS_V + (size_t)head * 65536;
  const unsigned sm = lds_addr(smem);
  const int qt = qb * 8 + w;
  const bf16x8 qf = *(const bf16x8*)(ws_c + WS_Q + ((size_t)head * 64 + qt) * 1024 + l * 16);
  if (tid < 3 * 8 * 8) {
    const int t = tid >> 3;
    *(LDSP(u32x4))(smem + (t >> 3) * AT_CH + 8192 + (t & 7) * 1152 + 1024 + (tid & 7) * 16) =
        u32x4{0x3F803F80u, 0x3F803F80u, 0x3F803F80u, 0x3F803F80u};
  }
#define AT_STAGE(ch, buf)                                                      \
  do {                                                                         \
    glds16(kg + ((ch) * 8 + w) * 1024, l * 16, sm + (buf) * AT_CH + w * 1024); \
    glds16(vg + ((ch) * 8 + w) * 1024, l * 16, sm + (buf) * AT_CH + 8192 + w * 1152);\
  } while (0)
  AT_STAGE(0, 0);
  AT_STAGE(1, 1);
  const int d = l & 31, ha = l >> 5;
  const int cst = 8192 + 1024 + ((d >= 20 && d < 28) ? 64 : 0);
  const int va0 = d < 16 ? 8192 + (ha * 16 + d) * 16 : cst;
  const int va1 = d < 16 ? va0 + 512 : cst;
  f32x16 acc = {};
  int buf = 0;
  for (int ch = 0; ch < 8; ch++) {
    if (ch < 7) WAIT_V(2); else WAIT_V(0);
    BAR();
    if (ch + 2 < 8) { const int nb = buf >= 1 ? buf - 1 : 2; AT_STAGE(ch + 2, nb); }
    const char* cb = smem + buf * AT_CH;
    const char* kp = cb + l * 16;
    const char* v0 = cb + va0;
    const char* v1 = cb + va1;
#pragma unroll
    for (int t = 0; t < 8; t++) {
      const bf16x8 kf = lds_b128(kp + t * 1024);
      const bf16x8 x0 = lds_b128(v0 + t * 1152), x1 = lds_b128(v1 + t * 1152);
      f32x16 s = mfma32(kf, qf, f32x16{});
      f32x8 p0, p1;
#pragma unroll
      for (int j = 0; j < 8; j++) { p0[j] = __builtin_amdgcn_exp2f(s[j]); p1[j] = __builtin_amdgcn_exp2f(s[8 + j]); }
      acc = pv_step(x0, p0, acc);
      acc = pv_step(x1, p1, acc);
    }
    buf = buf == 2 ? 0 : buf + 1;
  }
#undef AT_STAGE
  float lsum = acc[8];
  bool ok = lsum >= 0x1p-40f && lsum <= 0x1p40f;
#pragma unroll
  for (int j = 0; j < 8; j++) ok = ok && (__builtin_fabsf(acc[j]) < 0x1p120f);
  if (__builtin_amdgcn_ballot_w64(!ok) != 0) {
    float m = -__builtin_inff();
    acc = f32x16{};
    const bf16x8 ones = __builtin_bit_cast(bf16x8, u32x4{0x3F803F80u, 0x3F803F80u, 0x3F803F80u, 0x3F803F80u});
    const char* vl = ws_c + WS_V + (size_t)head * 65536 + (ha * 16 + (d & 15)) * 16;
    for (int t = 0; t < 64; t++) {
      const bf16x8 kf = *(const bf16x8*)(kg + t * 1024 + l * 16);
      bf16x8 x0 = *(const bf16x8*)(vl + t * 1024), x1 = *(const bf16x8*)(vl + t * 1024 + 512);
      if (d >= 16) { x0 = ones; x1 = ones; }
      f32x16 s = mfma32(kf, qf, f32x16{});
      float tm = s[0];
#pragma unroll
      for (int j = 1; j < 16; j++) tm = __builtin_fmaxf(tm, s[j]);
      tm = __builtin_fmaxf(tm, __shfl_xor(tm, 32));
      const float mn = __builtin_fmaxf(m, tm);
      const float al = __builtin_amdgcn_exp2f(m - mn);
      m = mn;
      f32x8 p0, p1;
#pragma unroll
      for (int j = 0; j < 8; j++) { p0[j] = __builtin_amdgcn_exp2f(s[j] - mn); p1[j] = __builtin_amdgcn_exp2f(s[8 + j] - mn); }
#pragma unroll
      for (int j = 0; j < 16; j++) acc[j] *= al;
      acc = pv_step(x0, p0, acc);
      acc = pv_step(x1, p1, acc);
    }
    lsum = acc[8];
  }
  const float inv = 1.0f / lsum;
  u32x4 o;
#pragma unroll
  for (int j = 0; j < 4; j++) o[j] = pk_bf16(acc[2 * j] * inv, acc[2 * j + 1] * inv);
  *(u32x4*)(ws + WS_O + ((size_t)qt * 32 + head) * 1024 + l * 16) = o;
}

__global__ __launch_bounds__(512, 2) void k2_attn(char* __restrict__ ws) {
  __shared__ __attribute__((aligned(16))) char smem[AT_LDS];
  const int b = blockIdx.x;
  attn_body(smem, ws, ws, (b & 7) * 4 + ((b >> 3) & 3), b >> 5);
}

constexpr int GU_ST = 8192 + 16384;
constexpr int GU_LDS = 3 * GU_ST;

DEV void gateup_body(char* smem, const u16* __restrict__ w1, const u16* __restrict__ w3, char* ws, int mb, int nb) {
  const int tid = threadIdx.x, l = tid & 63;
  const int w = __builtin_amdgcn_readfirstlane(tid >> 6);
  const char* ag = ws + WS_XN + ((size_t)(mb * 4 + (w >> 1)) * 32 + (w & 1)) * 1024;
  const unsigned sm = lds_addr(smem);
  const u16* bg = ((w & 1) ? w3 : w1) + (size_t)((w >> 1) * 8) * F + nb * 128;
  const unsigned bvo = (((l & 31) >> 2) * F + (l >> 5) * 32 + (l & 3) * 8) * 2;
#define GU_STAGE(s, buf)                                                       \
  do {                                                                         \
    glds16(ag + (s) * 2048, l * 16, sm + (buf) * GU_ST + w * 1024);            \
    const u16* p_ = bg + (size_t)(s) * 32 * F;                                 \
    glds16(p_, bvo, sm + (buf) * GU_ST + 8192 + w * 2048);                     \
    glds16(p_ + 64, bvo, sm + (buf) * GU_ST + 8192 + w * 2048 + 1024);         \
  } while (0)
  GU_STAGE(0, 0);
  GU_STAGE(1, 1);
  const int mw = w & 1, nw = w >> 1, h = l >> 5;
  const int aoff = mw * 4096 + l * 16;
  const int boff = 8192 + h * 4096 + nw * 512 + ((l & 15) >> 2) * 64 + ((l >> 4) & 1) * 32 + (l & 3) * 8;
  f32x16 ga[2] = {}, ua[2] = {};
  int buf = 0;
  for (int s = 0; s < 16; s++) {
    if (s < 15) WAIT_V(3); else WAIT_V(0);
    BAR();
    if (s + 2 < 16) { const int nbuf = buf >= 1 ? buf - 1 : 2; GU_STAGE(s + 2, nbuf); }
    const char* sb = smem + buf * GU_ST;
#pragma unroll
    for (int ks = 0; ks < 2; ks++) {
      const bf16x8 a0 = lds_b128(sb + aoff + ks * 1024), a1 = lds_b128(sb + aoff + 2048 + ks * 1024);
      const char* bp = sb + boff + ks * 8192;
      const bf16x8 bg_ = lds_tr2(bp, bp + 256), bu_ = lds_tr2(bp + 2048, bp + 2304);
      ga[0] = mfma32(bg_, a0, ga[0]); ua[0] = mfma32(bu_, a0, ua[0]);
      ga[1] = mfma32(bg_, a1, ga[1]); ua[1] = mfma32(bu_, a1, ua[1]);
    }
    buf = buf == 2 ? 0 : buf + 1;
  }
#undef GU_STAGE
#pragma unroll
  for (int a = 0; a < 2; a++) {
    const int mblk = mb * 4 + mw * 2 + a;
#pragma unroll
    for (int hs = 0; hs < 2; hs++) {
      u32x4 o;
#pragma unroll
      for (int j = 0; j < 4; j++) {
        float r[2];
#pragma unroll
        for (int e = 0; e < 2; e++) {
          const float gv = ga[a][hs * 8 + 2 * j + e], uv = ua[a][hs * 8 + 2 * j + e];
          const float sg = __builtin_amdgcn_rcpf(1.0f + __builtin_amdgcn_exp2f(gv * -1.4426950408889634f));
          r[e] = gv * sg * uv;
        }
        o[j] = pk_bf16(r[0], r[1]);
      }
      const int ks = nb * 8 + nw * 2 + hs;
      *(u32x4*)(ws + WS_H + ((size_t)mblk * 112 + ks) * 1024 + l * 16) = o;
    }
  }
}

__global__ __launch_bounds__(512, 2) void k2_gateup(const u16* __restrict__ w1, const u16* __restrict__ w3, char* __restrict__ ws) {
  __shared__ __attribute__((aligned(16))) char smem[GU_LDS];
  const int b = blockIdx.x;
  gateup_body(smem, w1, w3, ws, b / 14, b % 14);
}

constexpr int FN_ST = 32768;
constexpr int FN_LDS = 3 * FN_ST;

__global__ __launch_bounds__(512, 2) void k3_final(const u16* __restrict__ x, const u16* __restrict__ w2, const u16* __restrict__ wo,
                                                   const char* __restrict__ ws, u16* __restrict__ out) {
  __shared__ __attribute__((aligned(16))) char smem[FN_LDS];
  const int tid = threadIdx.x, l = tid & 63;
  const int w = __builtin_amdgcn_readfirstlane(tid >> 6);
  const int b = blockIdx.x, nb = (b >> 3) & 7, mb = (b & 7) * 4 + (b >> 6);
  const char* ah = ws + WS_H + ((size_t)(mb * 2 + (w >> 2)) * 112 + (w & 3) * 2) * 1024;
  const char* ao = ws + WS_O + ((size_t)(mb * 2 + (w >> 2)) * 32 + (w & 3) * 2) * 1024;
  const size_t brow = (size_t)(w * 16) * D + nb * 64;
  const unsigned bvo = (((l & 31) >> 2) * D + (l >> 5) * 32 + (l & 3) * 8) * 2;
  const unsigned sm = lds_addr(smem);
#define FN_STAGE(s, buf)                                                       \
  do {                                                                         \
    const char* a_ = (s) < 14 ? ah + (s) * 8192 : ao + ((s) - 14) * 8192;      \
    const u16* p_ = ((s) < 14 ? w2 + (size_t)(s) * 128 * D : wo + (size_t)((s) - 14) * 128 * D) + brow; \
    const unsigned d_ = sm + (buf) * FN_ST;                                    \
    glds16(a_, l * 16, d_ + (w >> 2) * 8192 + (w & 3) * 2048);                 \
    glds16(a_ + 1024, l * 16, d_ + (w >> 2) * 8192 + (w & 3) * 2048 + 1024);   \
    glds16(p_, bvo, d_ + 16384 + w * 2048);                                    \
    glds16(p_ + 8 * D, bvo, d_ + 16384 + w * 2048 + 1024);                     \
  } while (0)
  FN_STAGE(0, 0);
  FN_STAGE(1, 1);
  const int nh = w & 1, kq = w >> 1, h = l >> 5;
  const int aoff = kq * 2048 + l * 16;
  const int boff = 16384 + kq * 4096 + nh * 512 + (4 * h + ((l & 15) >> 2)) * 64 + ((l >> 4) & 1) * 32 + (l & 3) * 8;
  f32x16 acc[2] = {};
  int buf = 0;
  for (int s = 0; s < 18; s++) {
    if (s < 17) WAIT_V(4); else WAIT_V(0);
    BAR();
    if (s + 2 < 18) { const int nbuf = buf >= 1 ? buf - 1 : 2; FN_STAGE(s + 2, nbuf); }
    const char* sb = smem + buf * FN_ST;
#pragma unroll
    for (int ks = 0; ks < 2; ks++) {
      const bf16x8 a0 = lds_b128(sb + aoff + ks * 1024), a1 = lds_b128(sb + aoff + 8192 + ks * 1024);
      const char* bp = sb + boff + ks * 2048;
      const bf16x8 bw = lds_tr2(bp, bp + 1024);
      acc[0] = mfma32(bw, a0, acc[0]);
      acc[1] = mfma32(bw, a1, acc[1]);
    }
    buf = buf == 2 ? 0 : buf + 1;
  }
#undef FN_STAGE
  BAR();
#pragma unroll
  for (int a = 0; a < 2; a++)
#pragma unroll
    for (int j = 0; j < 4; j++) {
      f32x4 v = {acc[a][4 * j], acc[a][4 * j + 1], acc[a][4 * j + 2], acc[a][4 * j + 3]};
      *(LDSP(f32x4))(smem + ((kq * 64 + a * 32 + (l & 31)) * 68 + nh * 32 + 8 * j + 4 * h) * 4) = v;
    }
  BAR();
  const int tok = tid >> 3, n8 = (tid & 7) * 8;
  const size_t go = (size_t)(mb * 64 + tok) * D + nb * 64 + n8;
  const u32x4 xv = *(const u32x4*)(x + go);
  f32x4 s0 = {}, s1 = {};
#pragma unroll
  for (int q = 0; q < 4; q++) {
    const char* p = smem + ((q * 64 + tok) * 68 + n8) * 4;
    s0 += *(const LDSP(f32x4))p;
    s1 += *(const LDSP(f32x4))(p + 16);
  }
  u32x4 o;
  o[0] = pk_bf16(s0[0] + bf_lo(xv[0]), s0[1] + bf_hi(xv[0]));
  o[1] = pk_bf16(s0[2] + bf_lo(xv[1]), s0[3] + bf_hi(xv[1]));
  o[2] = pk_bf16(s1[0] + bf_lo(xv[2]), s1[1] + bf_hi(xv[2]));
  o[3] = pk_bf16(s1[2] + bf_lo(xv[3]), s1[3] + bf_hi(xv[3]));
  *(u32x4*)(out + go) = o;
}

extern "C" void kernel_launch(void* const* d_in, const int* in_sizes, int n_in, void* d_out, int out_size, void* d_ws,
                              size_t ws_size, hipStream_t stream) {
  const u16* x = (const u16*)d_in[0];
  const u16* wqkv = (const u16*)d_in[1];
  const u16* wo = (const u16*)d_in[2];
  const u16* w1 = (const u16*)d_in[4];
  const u16* w3 = (const u16*)d_in[5];
  const u16* w2 = (const u16*)d_in[6];
  char* ws = (char*)d_ws;
  k1_ln_qkv<<<256, 512, 0, stream>>>(x, wqkv, ws);
  k2_attn<<<256, 512, 0, stream>>>(ws);
  k2_gateup<<<224, 512, 0, stream>>>(w1, w3, ws);
  k3_final<<<256, 512, 0, stream>>>(x, w2, wo, ws, (u16*)d_out);
}
```

```cpp
#include <hip/hip_runtime.h>
#include <stdint.h>

typedef __bf16 bf16x2 __attribute__((ext_vector_type(2)));
typedef __bf16 bf16x4 __attribute__((ext_vector_type(4)));
typedef __bf16 bf16x8 __attribute__((ext_vector_type(8)));
typedef __bf16 bf16x16 __attribute__((ext_vector_type(16)));
typedef float f32x2 __attribute__((ext_vector_type(2)));
typedef float f32x4 __attribute__((ext_vector_type(4)));
typedef float f32x8 __attribute__((ext_vector_type(8)));
typedef float f32x16 __attribute__((ext_vector_type(16)));
typedef short s16x4 __attribute__((ext_vector_type(4)));
typedef unsigned u32x2 __attribute__((ext_vector_type(2)));
typedef unsigned u32x4 __attribute__((ext_vector_type(4)));
typedef unsigned short u16;

#define DEV __device__ __forceinline__
#define LDSP(T) __attribute__((address_space(3))) T*
#define WAIT_V(n) asm volatile("s_waitcnt vmcnt(" #n ")" ::: "memory")
#define WAIT_L(n) asm volatile("s_waitcnt lgkmcnt(" #n ")" ::: "memory")
#define BAR() do { asm volatile("s_waitcnt lgkmcnt(0)" ::: "memory"); __builtin_amdgcn_s_barrier(); asm volatile("" ::: "memory"); } while (0)

constexpr int S = 2048, D = 512, NH = 32, F = 1792;
constexpr size_t WS_XN = 0;
constexpr size_t WS_Q = WS_XN + 2u * S * D;
constexpr size_t WS_K = WS_Q + 2u * S * D;
constexpr size_t WS_V = WS_K + 2u * S * D;
constexpr size_t WS_O = WS_V + 2u * S * D;
constexpr size_t WS_H = WS_O + 2u * S * D;

DEV void glds16(const void* base, unsigned voff, unsigned lds) {
  unsigned keep;
  asm volatile("s_mov_b32 %0, m0\n\ts_mov_b32 m0, %3\n\ts_nop 0\n\tglobal_load_lds_dwordx4 %1, %2\n\ts_mov_b32 m0, %0"
               : "=&s"(keep) : "v"(voff), "s"(base), "s"(lds) : "memory");
}
DEV unsigned lds_addr(const char* p) { return (unsigned)(uintptr_t)(const LDSP(char))p; }
DEV bf16x8 lds_b128(const char* p) { return *(const LDSP(bf16x8))p; }
DEV bf16x8 lds_tr2(const char* p0, const char* p1) {
  s16x4 a = __builtin_amdgcn_ds_read_tr16_b64_v4i16((LDSP(s16x4))p0);
  s16x4 b = __builtin_amdgcn_ds_read_tr16_b64_v4i16((LDSP(s16x4))p1);
  return __builtin_bit_cast(bf16x8, __builtin_shufflevector(a, b, 0, 1, 2, 3, 4, 5, 6, 7));
}
DEV unsigned pk_bf16(float a, float b) {
  f32x2 v = {a, b};
  return __builtin_bit_cast(unsigned, __builtin_convertvector(v, bf16x2));
}
DEV float bf_lo(unsigned u) { return __builtin_bit_cast(float, u << 16); }
DEV float bf_hi(unsigned u) { return __builtin_bit_cast(float, u & 0xffff0000u); }
template <int CTRL> DEV float dpp_add(float v) {
  return v + __builtin_bit_cast(float, __builtin_amdgcn_update_dpp(0, __builtin_bit_cast(int, v), CTRL, 0xf, 0xf, true));
}
DEV float row16_sum(float v) {
  v = dpp_add<0xB1>(v);
  v = dpp_add<0x4E>(v);
  v = dpp_add<0x141>(v);
  v = dpp_add<0x140>(v);
  return v;
}
DEV f32x16 mfma32(bf16x8 a, bf16x8 b, f32x16 c) { return __builtin_amdgcn_mfma_f32_32x32x16_bf16(a, b, c, 0, 0, 0); }
DEV f32x4 mfma16(bf16x8 a, bf16x8 b, f32x4 c) { return __builtin_amdgcn_mfma_f32_16x16x32_bf16(a, b, c, 0, 0, 0); }

constexpr int K1_A = 65536;
constexpr int K1_K8 = 3104;
constexpr int K1_ST = 8 * K1_K8;
constexpr int K1_LDS = K1_A + 3 * K1_ST;

__global__ __launch_bounds__(512, 2) void k1_ln_qkv(const u16* __restrict__ x, const u16* __restrict__ wqkv, char* __restrict__ ws) {
  __shared__ __attribute__((aligned(16))) char smem[K1_LDS];
  const int tid = threadIdx.x, l = tid & 63;
  const int w = __builtin_amdgcn_readfirstlane(tid >> 6);
  const int b = blockIdx.x, nb = b & 7, mb = b >> 3;
  const int r4 = l >> 4, c = l & 15;
  u32x4 xr[2][4];
#pragma unroll
  for (int g = 0; g < 2; g++)
#pragma unroll
    for (int i = 0; i < 4; i++)
      xr[g][i] = *(const u32x4*)(x + (size_t)(mb * 64 + w * 8 + g * 4 + r4) * D + i * 128 + c * 8);
  const u16* wsrc = wqkv + (size_t)(w * 8) * (3 * D) + nb * 64;
  const unsigned wvo = (((l & 31) >> 2) * (3 * D) + (l >> 5) * 32 + (l & 3) * 8) * 2;
  const unsigned bst = lds_addr(smem) + K1_A + w * K1_K8;
#define K1_STAGE(s, buf)                                                       \
  do {                                                                         \
    const u16* p_ = wsrc + (size_t)(s) * 64 * (3 * D);                         \
    glds16(p_, wvo, bst + (buf) * K1_ST);                                      \
    glds16(p_ + D, wvo, bst + (buf) * K1_ST + 1024);                           \
    glds16(p_ + 2 * D, wvo, bst + (buf) * K1_ST + 2048);                       \
  } while (0)
  K1_STAGE(0, 0);
  K1_STAGE(1, 1);
#pragma unroll
  for (int g = 0; g < 2; g++) {
    float v[32];
    float s = 0.f;
#pragma unroll
    for (int i = 0; i < 4; i++)
#pragma unroll
      for (int j = 0; j < 4; j++) {
        v[i * 8 + 2 * j] = bf_lo(xr[g][i][j]);
        v[i * 8 + 2 * j + 1] = bf_hi(xr[g][i][j]);
      }
#pragma unroll
    for (int j = 0; j < 32; j++) s += v[j];
    const float m = row16_sum(s) * (1.0f / D);
    float q = 0.f;
#pragma unroll
    for (int j = 0; j < 32; j++) { v[j] -= m; q += v[j] * v[j]; }
    const float rs = __builtin_amdgcn_rsqf(row16_sum(q) * (1.0f / D) + 1e-5f);
    const int row = w * 8 + g * 4 + r4, tok = row & 31;
#pragma unroll
    for (int i = 0; i < 4; i++) {
      u32x4 o;
#pragma unroll
      for (int j = 0; j < 4; j++) o[j] = pk_bf16(v[i * 8 + 2 * j] * rs, v[i * 8 + 2 * j + 1] * rs);
      const int ks = 8 * i + (c >> 1), half = c & 1;
      *(LDSP(u32x4))(smem + (row >> 5) * 32768 + (ks * 64 + half * 32 + (tok ^ (c & 7))) * 16) = o;
      if (2 * i + (c >> 3) == nb)
        *(u32x4*)(ws + WS_XN + ((size_t)((mb * 2 + (row >> 5)) * 32 + ks) * 64 + half * 32 + tok) * 16) = o;
    }
  }
  const int mw = w & 1, nw = w >> 1, g = l >> 4;
  const char* ab[2];
#pragma unroll
  for (int p = 0; p < 2; p++)
    ab[p] = smem + mw * 32768 + (((g >> 1) * 64 + (g & 1) * 32) + ((l & 15) ^ (g | (4 * p)))) * 16;
  const char* bb = smem + K1_A + g * K1_K8 + (nw >> 1) * 512 + ((l & 15) >> 2) * 64 + (nw & 1) * 32 + (l & 3) * 8;
  f32x4 aq[2] = {}, ak[2] = {}, av[2] = {};
#pragma unroll
  for (int s = 0; s < 8; s++) {
    if (s < 7) WAIT_V(3); else WAIT_V(0);
    BAR();
    if (s + 2 < 8) K1_STAGE(s + 2, (s + 2) % 3);
#pragma unroll
    for (int p = 0; p < 2; p++) {
      const int ks32 = 2 * s + p;
      bf16x8 a0 = lds_b128(ab[p] + ks32 * 2048), a1 = lds_b128(ab[p] + ks32 * 2048 + 256);
      const char* bp = bb + (s % 3) * K1_ST + p * 4 * K1_K8;
      bf16x8 bq = lds_tr2(bp, bp + 256), bk = lds_tr2(bp + 1024, bp + 1280), bv = lds_tr2(bp + 2048, bp + 2304);
      aq[0] = mfma16(bq, a0, aq[0]); aq[1] = mfma16(bq, a1, aq[1]);
      ak[0] = mfma16(bk, a0, ak[0]); ak[1] = mfma16(bk, a1, ak[1]);
      av[0] = mfma16(a0, bv, av[0]); av[1] = mfma16(a1, bv, av[1]);
    }
  }
  const int head = nb * 4 + nw, tile = mb * 2 + mw;
  const size_t tb = ((size_t)head * 64 + tile) * 1024;
  const float qs = 0.25f * 1.4426950408889634f;
#pragma unroll
  for (int a = 0; a < 2; a++) {
    const size_t o = tb + ((g >> 1) * 32 + a * 16 + (l & 15)) * 16 + (g & 1) * 8;
    *(u32x2*)(ws + WS_Q + o) = u32x2{pk_bf16(aq[a][0] * qs, aq[a][1] * qs), pk_bf16(aq[a][2] * qs, aq[a][3] * qs)};
    *(u32x2*)(ws + WS_K + o) = u32x2{pk_bf16(ak[a][0], ak[a][1]), pk_bf16(ak[a][2], ak[a][3])};
    const size_t ov = tb + a * 512 + ((g >> 1) * 16 + (l & 15)) * 16 + (g & 1) * 8;
    *(u32x2*)(ws + WS_V + ov) = u32x2{pk_bf16(av[a][0], av[a][1]), pk_bf16(av[a][2], av[a][3])};
  }
#undef K1_STAGE
}

constexpr int AT_CH = 8 * 1024 + 8 * 1152;
constexpr int AT_LDS = 3 * AT_CH;

DEV f32x16 pv_step(bf16x8 v, f32x8 p, f32x16 acc) {
  return __builtin_amdgcn_smfmac_f32_32x32x32_bf16(v, __builtin_bit_cast(bf16x16, p), acc, (int)0xDDDDDDDD, 0, 0);
}

DEV void attn_body(char* smem, const char* ws_c, char* ws, int head, int qb) {
  const int tid = threadIdx.x, l = tid & 63;
  const int w = __builtin_amdgcn_readfirstlane(tid >> 6);
  const char* kg = ws_c + WS_K + (size_t)head * 65536;
  const char* vg = ws_c + WS_V + (size_t)head * 65536;
  const unsigned sm = lds_addr(smem);
  const int qt = qb * 8 + w;
  const bf16x8 qf = *(const bf16x8*)(ws_c + WS_Q + ((size_t)head * 64 + qt) * 1024 + l * 16);
  if (tid < 3 * 8 * 8) {
    const int t = tid >> 3;
    *(LDSP(u32x4))(smem + (t >> 3) * AT_CH + 8192 + (t & 7) * 1152 + 1024 + (tid & 7) * 16) =
        u32x4{0x3F803F80u, 0x3F803F80u, 0x3F803F80u, 0x3F803F80u};
  }
#define AT_STAGE(ch, buf)                                                      \
  do {                                                                         \
    glds16(kg + ((ch) * 8 + w) * 1024, l * 16, sm + (buf) * AT_CH + w * 1024); \
    glds16(vg + ((ch) * 8 + w) * 1024, l * 16, sm + (buf) * AT_CH + 8192 + w * 1152);\
  } while (0)
  AT_STAGE(0, 0);
  AT_STAGE(1, 1);
  const int d = l & 31, ha = l >> 5;
  const int cst = 8192 + 1024 + ((d >= 20 && d < 28) ? 64 : 0);
  const int va0 = d < 16 ? 8192 + (ha * 16 + d) * 16 : cst;
  const int va1 = d < 16 ? va0 + 512 : cst;
  f32x16 acc = {};
  WAIT_V(2);
  BAR();
  f32x16 sA = mfma32(lds_b128(smem + l * 16), qf, f32x16{});
  bf16x8 kn = lds_b128(smem + l * 16 + 1024);
  bf16x8 x0 = lds_b128(smem + va0), x1 = lds_b128(smem + va1);
  int buf = 0;
  for (int ch = 0; ch < 8; ch++) {
    const int nbuf = buf == 2 ? 0 : buf + 1;
    const char* cb = smem + buf * AT_CH;
    const char* cn = smem + (ch < 7 ? nbuf : buf) * AT_CH;
#pragma unroll
    for (int t = 0; t < 8; t++) {
      if (t == 6) {
        WAIT_V(0);
        BAR();
        if (ch + 2 < 8) { const int nb = buf >= 1 ? buf - 1 : 2; AT_STAGE(ch + 2, nb); }
      }
      f32x8 p0, p1;
#pragma unroll
      for (int j = 0; j < 4; j++) p0[j] = __builtin_amdgcn_exp2f(sA[j]);
      f32x16 sB = mfma32(kn, qf, f32x16{});
      kn = lds_b128((t + 2 < 8 ? cb + (t + 2) * 1024 : cn + (t + 2 - 8) * 1024) + l * 16);
#pragma unroll
      for (int j = 4; j < 8; j++) p0[j] = __builtin_amdgcn_exp2f(sA[j]);
      acc = pv_step(x0, p0, acc);
      x0 = lds_b128((t + 1 < 8 ? cb + (t + 1) * 1152 : cn) + va0);
#pragma unroll
      for (int j = 0; j < 8; j++) p1[j] = __builtin_amdgcn_exp2f(sA[8 + j]);
      acc = pv_step(x1, p1, acc);
      x1 = lds_b128((t + 1 < 8 ? cb + (t + 1) * 1152 : cn) + va1);
      sA = sB;
      __builtin_amdgcn_sched_group_barrier(0x400, 4, 0);
      __builtin_amdgcn_sched_group_barrier(0x008, 1, 0);
      __builtin_amdgcn_sched_group_barrier(0x100, 1, 0);
      __builtin_amdgcn_sched_group_barrier(0x400, 4, 0);
      __builtin_amdgcn_sched_group_barrier(0x008, 1, 0);
      __builtin_amdgcn_sched_group_barrier(0x100, 1, 0);
      __builtin_amdgcn_sched_group_barrier(0x400, 8, 0);
      __builtin_amdgcn_sched_group_barrier(0x008, 1, 0);
      __builtin_amdgcn_sched_group_barrier(0x100, 1, 0);
    }
    buf = nbuf;
  }
#undef AT_STAGE
  float lsum = acc[8];
  bool ok = lsum >= 0x1p-40f && lsum <= 0x1p40f;
#pragma unroll
  for (int j = 0; j < 8; j++) ok = ok && (__builtin_fabsf(acc[j]) < 0x1p120f);
  if (__builtin_amdgcn_ballot_w64(!ok) != 0) {
    float m = -__builtin_inff();
    acc = f32x16{};
    const bf16x8 ones = __builtin_bit_cast(bf16x8, u32x4{0x3F803F80u, 0x3F803F80u, 0x3F803F80u, 0x3F803F80u});
    const char* vl = ws_c + WS_V + (size_t)head * 65536 + (ha * 16 + (d & 15)) * 16;
    for (int t = 0; t < 64; t++) {
      const bf16x8 kf = *(const bf16x8*)(kg + t * 1024 + l * 16);
      bf16x8 x0 = *(const bf16x8*)(vl + t * 1024), x1 = *(const bf16x8*)(vl + t * 1024 + 512);
      if (d >= 16) { x0 = ones; x1 = ones; }
      f32x16 s = mfma32(kf, qf, f32x16{});
      float tm = s[0];
#pragma unroll
      for (int j = 1; j < 16; j++) tm = __builtin_fmaxf(tm, s[j]);
      tm = __builtin_fmaxf(tm, __shfl_xor(tm, 32));
      const float mn = __builtin_fmaxf(m, tm);
      const float al = __builtin_amdgcn_exp2f(m - mn);
      m = mn;
      f32x8 p0, p1;
#pragma unroll
      for (int j = 0; j < 8; j++) { p0[j] = __builtin_amdgcn_exp2f(s[j] - mn); p1[j] = __builtin_amdgcn_exp2f(s[8 + j] - mn); }
#pragma unroll
      for (int j = 0; j < 16; j++) acc[j] *= al;
      acc = pv_step(x0, p0, acc);
      acc = pv_step(x1, p1, acc);
    }
    lsum = acc[8];
  }
  const float inv = 1.0f / lsum;
  u32x4 o;
#pragma unroll
  for (int j = 0; j < 4; j++) o[j] = pk_bf16(acc[2 * j] * inv, acc[2 * j + 1] * inv);
  *(u32x4*)(ws + WS_O + ((size_t)qt * 32 + head) * 1024 + l * 16) = o;
}

__global__ __launch_bounds__(512, 2) void k2_attn(char* __restrict__ ws) {
  __shared__ __attribute__((aligned(16))) char smem[AT_LDS];
  const int b = blockIdx.x;
  attn_body(smem, ws, ws, (b & 7) * 4 + ((b >> 3) & 3), b >> 5);
}

constexpr int GU_ST = 8192 + 16384;
constexpr int GU_LDS = 3 * GU_ST;

DEV void gateup_body(char* smem, const u16* __restrict__ w1, const u16* __restrict__ w3, char* ws, int mb, int nb) {
  const int tid = threadIdx.x, l = tid & 63;
  const int w = __builtin_amdgcn_readfirstlane(tid >> 6);
  const char* ag = ws + WS_XN + ((size_t)(mb * 4 + (w >> 1)) * 32 + (w & 1)) * 1024;
  const unsigned sm = lds_addr(smem);
  const u16* bg = ((w & 1) ? w3 : w1) + (size_t)((w >> 1) * 8) * F + nb * 128;
  const unsigned bvo = (((l & 31) >> 2) * F + (l >> 5) * 32 + (l & 3) * 8) * 2;
#define GU_STAGE(s, buf)                                                       \
  do {                                                                         \
    glds16(ag + (s) * 2048, l * 16, sm + (buf) * GU_ST + w * 1024);            \
    const u16* p_ = bg + (size_t)(s) * 32 * F;                                 \
    glds16(p_, bvo, sm + (buf) * GU_ST + 8192 + w * 2048);                     \
    glds16(p_ + 64, bvo, sm + (buf) * GU_ST + 8192 + w * 2048 + 1024);         \
  } while (0)
  GU_STAGE(0, 0);
  GU_STAGE(1, 1);
  const int mw = w & 1, nw = w >> 1, h = l >> 5;
  const int aoff = mw * 4096 + l * 16;
  const int boff = 8192 + h * 4096 + nw * 512 + ((l & 15) >> 2) * 64 + ((l >> 4) & 1) * 32 + (l & 3) * 8;
  f32x16 ga[2] = {}, ua[2] = {};
  int buf = 0;
  for (int s = 0; s < 16; s++) {
    if (s < 15) WAIT_V(3); else WAIT_V(0);
    BAR();
    if (s + 2 < 16) { const int nbuf = buf >= 1 ? buf - 1 : 2; GU_STAGE(s + 2, nbuf); }
    const char* sb = smem + buf * GU_ST;
#pragma unroll
    for (int ks = 0; ks < 2; ks++) {
      const bf16x8 a0 = lds_b128(sb + aoff + ks * 1024), a1 = lds_b128(sb + aoff + 2048 + ks * 1024);
      const char* bp = sb + boff + ks * 8192;
      const bf16x8 bg_ = lds_tr2(bp, bp + 256), bu_ = lds_tr2(bp + 2048, bp + 2304);
      ga[0] = mfma32(bg_, a0, ga[0]); ua[0] = mfma32(bu_, a0, ua[0]);
      ga[1] = mfma32(bg_, a1, ga[1]); ua[1] = mfma32(bu_, a1, ua[1]);
    }
    buf = buf == 2 ? 0 : buf + 1;
  }
#undef GU_STAGE
#pragma unroll
  for (int a = 0; a < 2; a++) {
    const int mblk = mb * 4 + mw * 2 + a;
#pragma unroll
    for (int hs = 0; hs < 2; hs++) {
      u32x4 o;
#pragma unroll
      for (int j = 0; j < 4; j++) {
        float r[2];
#pragma unroll
        for (int e = 0; e < 2; e++) {
          const float gv = ga[a][hs * 8 + 2 * j + e], uv = ua[a][hs * 8 + 2 * j + e];
          const float sg = __builtin_amdgcn_rcpf(1.0f + __builtin_amdgcn_exp2f(gv * -1.4426950408889634f));
          r[e] = gv * sg * uv;
        }
        o[j] = pk_bf16(r[0], r[1]);
      }
      const int ks = nb * 8 + nw * 2 + hs;
      *(u32x4*)(ws + WS_H + ((size_t)mblk * 112 + ks) * 1024 + l * 16) = o;
    }
  }
}

__global__ __launch_bounds__(512, 2) void k2_gateup(const u16* __restrict__ w1, const u16* __restrict__ w3, char* __restrict__ ws) {
  __shared__ __attribute__((aligned(16))) char smem[GU_LDS];
  const int b = blockIdx.x;
  gateup_body(smem, w1, w3, ws, b / 14, b % 14);
}

constexpr int FN_ST = 32768;
constexpr int FN_LDS = 3 * FN_ST;

__global__ __launch_bounds__(512, 2) void k3_final(const u16* __restrict__ x, const u16* __restrict__ w2, const u16* __restrict__ wo,
                                                   char* __restrict__ ws, u16* __restrict__ out) {
  __shared__ __attribute__((aligned(16))) char smem[FN_LDS];
  const int tid = threadIdx.x, l = tid & 63;
  const int w = __builtin_amdgcn_readfirstlane(tid >> 6);
  const int b = blockIdx.x, nb = (b >> 3) & 7, mb = (b & 7) * 4 + (b >> 6);
  const char* ah = ws + WS_H + ((size_t)(mb * 2 + (w >> 2)) * 112 + (w & 3) * 2) * 1024;
  const char* ao = ws + WS_O + ((size_t)(mb * 2 + (w >> 2)) * 32 + (w & 3) * 2) * 1024;
  const size_t brow = (size_t)(w * 16) * D + nb * 64;
  const unsigned bvo = (((l & 31) >> 2) * D + (l >> 5) * 32 + (l & 3) * 8) * 2;
  const unsigned sm = lds_addr(smem);
#define FN_STAGE(s, buf)                                                       \
  do {                                                                         \
    const char* a_ = (s) < 14 ? ah + (s) * 8192 : ao + ((s) - 14) * 8192;      \
    const u16* p_ = ((s) < 14 ? w2 + (size_t)(s) * 128 * D : wo + (size_t)((s) - 14) * 128 * D) + brow; \
    const unsigned d_ = sm + (buf) * FN_ST;                                    \
    glds16(a_, l * 16, d_ + (w >> 2) * 8192 + (w & 3) * 2048);                 \
    glds16(a_ + 1024, l * 16, d_ + (w >> 2) * 8192 + (w & 3) * 2048 + 1024);   \
    glds16(p_, bvo, d_ + 16384 + w * 2048);                                    \
    glds16(p_ + 8 * D, bvo, d_ + 16384 + w * 2048 + 1024);                     \
  } while (0)
  FN_STAGE(0, 0);
  FN_STAGE(1, 1);
  const int nh = w & 1, kq = w >> 1, h = l >> 5;
  const int aoff = kq * 2048 + l * 16;
  const int boff = 16384 + kq * 4096 + nh * 512 + (4 * h + ((l & 15) >> 2)) * 64 + ((l >> 4) & 1) * 32 + (l & 3) * 8;
  f32x16 acc[2] = {};
  int buf = 0;
  for (int s = 0; s < 18; s++) {
    if (s < 17) WAIT_V(4); else WAIT_V(0);
    BAR();
    if (s + 2 < 18) { const int nbuf = buf >= 1 ? buf - 1 : 2; FN_STAGE(s + 2, nbuf); }
    const char* sb = smem + buf * FN_ST;
#pragma unroll
    for (int ks = 0; ks < 2; ks++) {
      const bf16x8 a0 = lds_b128(sb + aoff + ks * 1024), a1 = lds_b128(sb + aoff + 8192 + ks * 1024);
      const char* bp = sb + boff + ks * 2048;
      const bf16x8 bw = lds_tr2(bp, bp + 1024);
      acc[0] = mfma32(bw, a0, acc[0]);
      acc[1] = mfma32(bw, a1, acc[1]);
    }
    buf = buf == 2 ? 0 : buf + 1;
  }
#undef FN_STAGE
  BAR();
#pragma unroll
  for (int a = 0; a < 2; a++)
#pragma unroll
    for (int j = 0; j < 4; j++) {
      f32x4 v = {acc[a][4 * j], acc[a][4 * j + 1], acc[a][4 * j + 2], acc[a][4 * j + 3]};
      *(LDSP(f32x4))(smem + ((kq * 64 + a * 32 + (l & 31)) * 68 + nh * 32 + 8 * j + 4 * h) * 4) = v;
    }
  BAR();
  const int tok = tid >> 3, n8 = (tid & 7) * 8;
  const size_t go = (size_t)(mb * 64 + tok) * D + nb * 64 + n8;
  const u32x4 xv = *(const u32x4*)(x + go);
  f32x4 s0 = {}, s1 = {};
#pragma unroll
  for (int q = 0; q < 4; q++) {
    const char* p = smem + ((q * 64 + tok) * 68 + n8) * 4;
    s0 += *(const LDSP(f32x4))p;
    s1 += *(const LDSP(f32x4))(p + 16);
  }
  u32x4 o;
  o[0] = pk_bf16(s0[0] + bf_lo(xv[0]), s0[1] + bf_hi(xv[0]));
  o[1] = pk_bf16(s0[2] + bf_lo(xv[1]), s0[3] + bf_hi(xv[1]));
  o[2] = pk_bf16(s1[0] + bf_lo(xv[2]), s1[1] + bf_hi(xv[2]));
  o[3] = pk_bf16(s1[2] + bf_lo(xv[3]), s1[3] + bf_hi(xv[3]));
  *(u32x4*)(out + go) = o;
}

extern "C" void kernel_launch(void* const* d_in, const int* in_sizes, int n_in, void* d_out, int out_size, void* d_ws,
                              size_t ws_size, hipStream_t stream) {
  const u16* x = (const u16*)d_in[0];
  const u16* wqkv = (const u16*)d_in[1];
  const u16* wo = (const u16*)d_in[2];
  const u16* w1 = (const u16*)d_in[4];
  const u16* w3 = (const u16*)d_in[5];
  const u16* w2 = (const u16*)d_in[6];
  char* ws = (char*)d_ws;
  k1_ln_qkv<<<256, 512, 0, stream>>>(x, wqkv, ws);
  k2_attn<<<256, 512, 0, stream>>>(ws);
  k2_gateup<<<224, 512, 0, stream>>>(w1, w3, ws);
  k3_final<<<256, 512, 0, stream>>>(x, w2, wo, ws, (u16*)d_out);
}
```

```cpp
#include <hip/hip_runtime.h>
#include <stdint.h>

typedef __bf16 bf16x2 __attribute__((ext_vector_type(2)));
typedef __bf16 bf16x4 __attribute__((ext_vector_type(4)));
typedef __bf16 bf16x8 __attribute__((ext_vector_type(8)));
typedef __bf16 bf16x16 __attribute__((ext_vector_type(16)));
typedef float f32x2 __attribute__((ext_vector_type(2)));
typedef float f32x4 __attribute__((ext_vector_type(4)));
typedef float f32x8 __attribute__((ext_vector_type(8)));
typedef float f32x16 __attribute__((ext_vector_type(16)));
typedef short s16x4 __attribute__((ext_vector_type(4)));
typedef unsigned u32x2 __attribute__((ext_vector_type(2)));
typedef unsigned u32x4 __attribute__((ext_vector_type(4)));
typedef unsigned short u16;

#define DEV __device__ __forceinline__
#define LDSP(T) __attribute__((address_space(3))) T*
#define WAIT_V(n) asm volatile("s_waitcnt vmcnt(" #n ")" ::: "memory")
#define WAIT_L(n) asm volatile("s_waitcnt lgkmcnt(" #n ")" ::: "memory")
#define BAR() do { asm volatile("s_waitcnt lgkmcnt(0)" ::: "memory"); __builtin_amdgcn_s_barrier(); asm volatile("" ::: "memory"); } while (0)

constexpr int S = 2048, D = 512, NH = 32, F = 1792;
constexpr size_t WS_XN = 0;
constexpr size_t WS_Q = WS_XN + 2u * S * D;
constexpr size_t WS_K = WS_Q + 2u * S * D;
constexpr size_t WS_V = WS_K + 2u * S * D;
constexpr size_t WS_O = WS_V + 2u * S * D;
constexpr size_t WS_H = WS_O + 2u * S * D;

DEV void glds16(const void* base, unsigned voff, unsigned lds) {
  unsigned keep;
  asm volatile("s_mov_b32 %0, m0\n\ts_mov_b32 m0, %3\n\ts_nop 0\n\tglobal_load_lds_dwordx4 %1, %2\n\ts_mov_b32 m0, %0"
               : "=&s"(keep) : "v"(voff), "s"(base), "s"(lds) : "memory");
}
DEV unsigned lds_addr(const char* p) { return (unsigned)(uintptr_t)(const LDSP(char))p; }
DEV bf16x8 lds_b128(const char* p) { return *(const LDSP(bf16x8))p; }
DEV bf16x8 lds_tr2(const char* p0, const char* p1) {
  s16x4 a = __builtin_amdgcn_ds_read_tr16_b64_v4i16((LDSP(s16x4))p0);
  s16x4 b = __builtin_amdgcn_ds_read_tr16_b64_v4i16((LDSP(s16x4))p1);
  return __builtin_bit_cast(bf16x8, __builtin_shufflevector(a, b, 0, 1, 2, 3, 4, 5, 6, 7));
}
DEV unsigned pk_bf16(float a, float b) {
  f32x2 v = {a, b};
  return __builtin_bit_cast(unsigned, __builtin_convertvector(v, bf16x2));
}
DEV float bf_lo(unsigned u) { return __builtin_bit_cast(float, u << 16); }
DEV float bf_hi(unsigned u) { return __builtin_bit_cast(float, u & 0xffff0000u); }
template <int CTRL> DEV float dpp_add(float v) {
  return v + __builtin_bit_cast(float, __builtin_amdgcn_update_dpp(0, __builtin_bit_cast(int, v), CTRL, 0xf, 0xf, true));
}
DEV float row16_sum(float v) {
  v = dpp_add<0xB1>(v);
  v = dpp_add<0x4E>(v);
  v = dpp_add<0x141>(v);
  v = dpp_add<0x140>(v);
  return v;
}
DEV f32x16 mfma32(bf16x8 a, bf16x8 b, f32x16 c) { return __builtin_amdgcn_mfma_f32_32x32x16_bf16(a, b, c, 0, 0, 0); }
DEV f32x4 mfma16(bf16x8 a, bf16x8 b, f32x4 c) { return __builtin_amdgcn_mfma_f32_16x16x32_bf16(a, b, c, 0, 0, 0); }

constexpr int K1_A = 65536;
constexpr int K1_K8 = 3104;
constexpr int K1_ST = 8 * K1_K8;
constexpr int K1_LDS = K1_A + 3 * K1_ST;

__global__ __launch_bounds__(512, 2) void k1_ln_qkv(const u16* __restrict__ x, const u16* __restrict__ wqkv, char* __restrict__ ws) {
  __shared__ __attribute__((aligned(16))) char smem[K1_LDS];
  const int tid = threadIdx.x, l = tid & 63;
  const int w = __builtin_amdgcn_readfirstlane(tid >> 6);
  const int b = blockIdx.x, nb = b & 7, mb = b >> 3;
  const int r4 = l >> 4, c = l & 15;
  u32x4 xr[2][4];
#pragma unroll
  for (int g = 0; g < 2; g++)
#pragma unroll
    for (int i = 0; i < 4; i++)
      xr[g][i] = *(const u32x4*)(x + (size_t)(mb * 64 + w * 8 + g * 4 + r4) * D + i * 128 + c * 8);
  const u16* wsrc = wqkv + (size_t)(w * 8) * (3 * D) + nb * 64;
  const unsigned wvo = (((l & 31) >> 2) * (3 * D) + (l >> 5) * 32 + (l & 3) * 8) * 2;
  const unsigned bst = lds_addr(smem) + K1_A + w * K1_K8;
#define K1_STAGE(s, buf)                                                       \
  do {                                                                         \
    const u16* p_ = wsrc + (size_t)(s) * 64 * (3 * D);                         \
    glds16(p_, wvo, bst + (buf) * K1_ST);                                      \
    glds16(p_ + D, wvo, bst + (buf) * K1_ST + 1024);                           \
    glds16(p_ + 2 * D, wvo, bst + (buf) * K1_ST + 2048);                       \
  } while (0)
  K1_STAGE(0, 0);
  K1_STAGE(1, 1);
#pragma unroll
  for (int g = 0; g < 2; g++) {
    float v[32];
    float s = 0.f;
#pragma unroll
    for (int i = 0; i < 4; i++)
#pragma unroll
      for (int j = 0; j < 4; j++) {
        v[i * 8 + 2 * j] = bf_lo(xr[g][i][j]);
        v[i * 8 + 2 * j + 1] = bf_hi(xr[g][i][j]);
      }
#pragma unroll
    for (int j = 0; j < 32; j++) s += v[j];
    const float m = row16_sum(s) * (1.0f / D);
    float q = 0.f;
#pragma unroll
    for (int j = 0; j < 32; j++) { v[j] -= m; q += v[j] * v[j]; }
    const float rs = __builtin_amdgcn_rsqf(row16_sum(q) * (1.0f / D) + 1e-5f);
    const int row = w * 8 + g * 4 + r4, tok = row & 31;
#pragma unroll
    for (int i = 0; i < 4; i++) {
      u32x4 o;
#pragma unroll
      for (int j = 0; j < 4; j++) o[j] = pk_bf16(v[i * 8 + 2 * j] * rs, v[i * 8 + 2 * j + 1] * rs);
      const int ks = 8 * i + (c >> 1), half = c & 1;
      *(LDSP(u32x4))(smem + (row >> 5) * 32768 + (ks * 64 + half * 32 + (tok ^ (c & 7))) * 16) = o;
      if (2 * i + (c >> 3) == nb)
        *(u32x4*)(ws + WS_XN + ((size_t)((mb * 2 + (row >> 5)) * 32 + ks) * 64 + half * 32 + tok) * 16) = o;
    }
  }
  const int mw = w & 1, nw = w >> 1, g = l >> 4;
  const char* ab[2];
#pragma unroll
  for (int p = 0; p < 2; p++)
    ab[p] = smem + mw * 32768 + (((g >> 1) * 64 + (g & 1) * 32) + ((l & 15) ^ (g | (4 * p)))) * 16;
  const char* bb = smem + K1_A + g * K1_K8 + (nw >> 1) * 512 + ((l & 15) >> 2) * 64 + (nw & 1) * 32 + (l & 3) * 8;
  f32x4 aq[2] = {}, ak[2] = {}, av[2] = {};
#pragma unroll
  for (int s = 0; s < 8; s++) {
    if (s < 7) WAIT_V(3); else WAIT_V(0);
    BAR();
    if (s + 2 < 8) K1_STAGE(s + 2, (s + 2) % 3);
#pragma unroll
    for (int p = 0; p < 2; p++) {
      const int ks32 = 2 * s + p;
      bf16x8 a0 = lds_b128(ab[p] + ks32 * 2048), a1 = lds_b128(ab[p] + ks32 * 2048 + 256);
      const char* bp = bb + (s % 3) * K1_ST + p * 4 * K1_K8;
      bf16x8 bq = lds_tr2(bp, bp + 256), bk = lds_tr2(bp + 1024, bp + 1280), bv = lds_tr2(bp + 2048, bp + 2304);
      aq[0] = mfma16(bq, a0, aq[0]); aq[1] = mfma16(bq, a1, aq[1]);
      ak[0] = mfma16(bk, a0, ak[0]); ak[1] = mfma16(bk, a1, ak[1]);
      av[0] = mfma16(a0, bv, av[0]); av[1] = mfma16(a1, bv, av[1]);
    }
  }
  const int head = nb * 4 + nw, tile = mb * 2 + mw;
  const size_t tb = ((size_t)head * 64 + tile) * 1024;
  const float qs = 0.25f * 1.4426950408889634f;
#pragma unroll
  for (int a = 0; a < 2; a++) {
    const size_t o = tb + ((g >> 1) * 32 + a * 16 + (l & 15)) * 16 + (g & 1) * 8;
    *(u32x2*)(ws + WS_Q + o) = u32x2{pk_bf16(aq[a][0] * qs, aq[a][1] * qs), pk_bf16(aq[a][2] * qs, aq[a][3] * qs)};
    *(u32x2*)(ws + WS_K + o) = u32x2{pk_bf16(ak[a][0], ak[a][1]), pk_bf16(ak[a][2], ak[a][3])};
    const size_t ov = tb + a * 512 + ((g >> 1) * 16 + (l & 15)) * 16 + (g & 1) * 8;
    *(u32x2*)(ws + WS_V + ov) = u32x2{pk_bf16(av[a][0], av[a][1]), pk_bf16(av[a][2], av[a][3])};
  }
#undef K1_STAGE
}

constexpr int AT_CH = 8 * 1024 + 8 * 1152;
constexpr int AT_LDS = 3 * AT_CH;

DEV f32x16 pv_step(bf16x8 v, f32x8 p, f32x16 acc) {
  return __builtin_amdgcn_smfmac_f32_32x32x32_bf16(v, __builtin_bit_cast(bf16x16, p), acc, (int)0xDDDDDDDD, 0, 0);
}

DEV void attn_body(char* smem, const char* ws_c, char* ws, int head, int qb) {
  const int tid = threadIdx.x, l = tid & 63;
  const int w = __builtin_amdgcn_readfirstlane(tid >> 6);
  const char* kg = ws_c + WS_K + (size_t)head * 65536;
  const char* vg = ws_c + WS_V + (size_t)head * 65536;
  const unsigned sm = lds_addr(smem);
  const int qt = qb * 8 + w;
  const bf16x8 qf = *(const bf16x8*)(ws_c + WS_Q + ((size_t)head * 64 + qt) * 1024 + l * 16);
  if (tid < 3 * 8 * 8) {
    const int t = tid >> 3;
    const unsigned o1 = (tid & 3) == 0 ? 0x3F803F80u : 0u;
    *(LDSP(u32x4))(smem + (t >> 3) * AT_CH + 8192 + (t & 7) * 1152 + 1024 + (tid & 7) * 16) = u32x4{o1, o1, o1, o1};
  }
#define AT_STAGE(ch, buf)                                                      \
  do {                                                                         \
    glds16(kg + ((ch) * 8 + w) * 1024, l * 16, sm + (buf) * AT_CH + w * 1024); \
    glds16(vg + ((ch) * 8 + w) * 1024, l * 16, sm + (buf) * AT_CH + 8192 + w * 1152);\
  } while (0)
  AT_STAGE(0, 0);
  AT_STAGE(1, 1);
  const int d = l & 31, ha = l >> 5;
  const int cst = 8192 + 1024 + ((d >= 20 && d < 28) ? 64 : 0) + ((d == 16 || d == 20) ? 0 : 16);
  const int va0 = d < 16 ? 8192 + (ha * 16 + d) * 16 : cst;
  const int va1 = d < 16 ? va0 + 512 : cst;
  f32x16 acc = {};
  WAIT_V(2);
  BAR();
  f32x16 sA = mfma32(lds_b128(smem + l * 16), qf, f32x16{});
  bf16x8 kn = lds_b128(smem + l * 16 + 1024);
  bf16x8 x0 = lds_b128(smem + va0), x1 = lds_b128(smem + va1);
  int buf = 0;
  for (int ch = 0; ch < 8; ch++) {
    const int nbuf = buf == 2 ? 0 : buf + 1;
    const char* cb = smem + buf * AT_CH;
    const char* cn = smem + (ch < 7 ? nbuf : buf) * AT_CH;
#pragma unroll
    for (int t = 0; t < 8; t++) {
      if (t == 6) {
        WAIT_V(0);
        BAR();
        if (ch + 2 < 8) { const int nb = buf >= 1 ? buf - 1 : 2; AT_STAGE(ch + 2, nb); }
      }
      f32x8 p0, p1;
#pragma unroll
      for (int j = 0; j < 4; j++) p0[j] = __builtin_amdgcn_exp2f(sA[j]);
      f32x16 sB = mfma32(kn, qf, f32x16{});
      kn = lds_b128((t + 2 < 8 ? cb + (t + 2) * 1024 : cn + (t + 2 - 8) * 1024) + l * 16);
#pragma unroll
      for (int j = 4; j < 8; j++) p0[j] = __builtin_amdgcn_exp2f(sA[j]);
      acc = pv_step(x0, p0, acc);
      x0 = lds_b128((t + 1 < 8 ? cb + (t + 1) * 1152 : cn) + va0);
#pragma unroll
      for (int j = 0; j < 8; j++) p1[j] = __builtin_amdgcn_exp2f(sA[8 + j]);
      acc = pv_step(x1, p1, acc);
      x1 = lds_b128((t + 1 < 8 ? cb + (t + 1) * 1152 : cn) + va1);
      sA = sB;
      __builtin_amdgcn_sched_group_barrier(0x400, 4, 0);
      __builtin_amdgcn_sched_group_barrier(0x008, 1, 0);
      __builtin_amdgcn_sched_group_barrier(0x100, 1, 0);
      __builtin_amdgcn_sched_group_barrier(0x400, 4, 0);
      __builtin_amdgcn_sched_group_barrier(0x008, 1, 0);
      __builtin_amdgcn_sched_group_barrier(0x100, 1, 0);
      __builtin_amdgcn_sched_group_barrier(0x400, 8, 0);
      __builtin_amdgcn_sched_group_barrier(0x008, 1, 0);
      __builtin_amdgcn_sched_group_barrier(0x100, 1, 0);
    }
    buf = nbuf;
  }
#undef AT_STAGE
  float lsum = acc[8];
  bool ok = lsum >= 0x1p-40f && lsum <= 0x1p40f;
#pragma unroll
  for (int j = 0; j < 8; j++) ok = ok && (__builtin_fabsf(acc[j]) < 0x1p120f);
  if (__builtin_amdgcn_ballot_w64(!ok) != 0) {
    float m = -__builtin_inff();
    acc = f32x16{};
    const bf16x8 ones = __builtin_bit_cast(bf16x8, u32x4{0x3F803F80u, 0x3F803F80u, 0x3F803F80u, 0x3F803F80u});
    const char* vl = ws_c + WS_V + (size_t)head * 65536 + (ha * 16 + (d & 15)) * 16;
    for (int t = 0; t < 64; t++) {
      const bf16x8 kf = *(const bf16x8*)(kg + t * 1024 + l * 16);
      bf16x8 x0 = *(const bf16x8*)(vl + t * 1024), x1 = *(const bf16x8*)(vl + t * 1024 + 512);
      if (d >= 16) { x0 = ones; x1 = ones; }
      f32x16 s = mfma32(kf, qf, f32x16{});
      float tm = s[0];
#pragma unroll
      for (int j = 1; j < 16; j++) tm = __builtin_fmaxf(tm, s[j]);
      tm = __builtin_fmaxf(tm, __shfl_xor(tm, 32));
      const float mn = __builtin_fmaxf(m, tm);
      const float al = __builtin_amdgcn_exp2f(m - mn);
      m = mn;
      f32x8 p0, p1;
#pragma unroll
      for (int j = 0; j < 8; j++) { p0[j] = __builtin_amdgcn_exp2f(s[j] - mn); p1[j] = __builtin_amdgcn_exp2f(s[8 + j] - mn); }
#pragma unroll
      for (int j = 0; j < 16; j++) acc[j] *= al;
      acc = pv_step(x0, p0, acc);
      acc = pv_step(x1, p1, acc);
    }
    lsum = acc[8];
  }
  const float inv = 1.0f / lsum;
  u32x4 o;
#pragma unroll
  for (int j = 0; j < 4; j++) o[j] = pk_bf16(acc[2 * j] * inv, acc[2 * j + 1] * inv);
  *(u32x4*)(ws + WS_O + ((size_t)qt * 32 + head) * 1024 + l * 16) = o;
}

constexpr int GU_ST = 8192 + 16384;
constexpr int GU_LDS = 3 * GU_ST;

DEV void gateup_body(char* smem, const u16* __restrict__ w1, const u16* __restrict__ w3, char* ws, int mb, int nb) {
  const int tid = threadIdx.x, l = tid & 63;
  const int w = __builtin_amdgcn_readfirstlane(tid >> 6);
  const char* ag = ws + WS_XN + ((size_t)(mb * 4 + (w >> 1)) * 32 + (w & 1)) * 1024;
  const unsigned sm = lds_addr(smem);
  const u16* bg = ((w & 1) ? w3 : w1) + (size_t)((w >> 1) * 8) * F + nb * 128;
  const unsigned bvo = (((l & 31) >> 2) * F + (l >> 5) * 32 + (l & 3) * 8) * 2;
#define GU_STAGE(s, buf)                                                       \
  do {                                                                         \
    glds16(ag + (s) * 2048, l * 16, sm + (buf) * GU_ST + w * 1024);            \
    const u16* p_ = bg + (size_t)(s) * 32 * F;                                 \
    glds16(p_, bvo, sm + (buf) * GU_ST + 8192 + w * 2048);                     \
    glds16(p_ + 64, bvo, sm + (buf) * GU_ST + 8192 + w * 2048 + 1024);         \
  } while (0)
  GU_STAGE(0, 0);
  GU_STAGE(1, 1);
  const int mw = w & 1, nw = w >> 1, h = l >> 5;
  const int aoff = mw * 4096 + l * 16;
  const int boff = 8192 + h * 4096 + nw * 512 + ((l & 15) >> 2) * 64 + ((l >> 4) & 1) * 32 + (l & 3) * 8;
  f32x16 ga[2] = {}, ua[2] = {};
  int buf = 0;
  for (int s = 0; s < 16; s++) {
    if (s < 15) WAIT_V(3); else WAIT_V(0);
    BAR();
    if (s + 2 < 16) { const int nbuf = buf >= 1 ? buf - 1 : 2; GU_STAGE(s + 2, nbuf); }
    const char* sb = smem + buf * GU_ST;
#pragma unroll
    for (int ks = 0; ks < 2; ks++) {
      const bf16x8 a0 = lds_b128(sb + aoff + ks * 1024), a1 = lds_b128(sb + aoff + 2048 + ks * 1024);
      const char* bp = sb + boff + ks * 8192;
      const bf16x8 bg_ = lds_tr2(bp, bp + 256), bu_ = lds_tr2(bp + 2048, bp + 2304);
      ga[0] = mfma32(bg_, a0, ga[0]); ua[0] = mfma32(bu_, a0, ua[0]);
      ga[1] = mfma32(bg_, a1, ga[1]); ua[1] = mfma32(bu_, a1, ua[1]);
    }
    buf = buf == 2 ? 0 : buf + 1;
  }
#undef GU_STAGE
#pragma unroll
  for (int a = 0; a < 2; a++) {
    const int mblk = mb * 4 + mw * 2 + a;
#pragma unroll
    for (int hs = 0; hs < 2; hs++) {
      u32x4 o;
#pragma unroll
      for (int j = 0; j < 4; j++) {
        float r[2];
#pragma unroll
        for (int e = 0; e < 2; e++) {
          const float gv = ga[a][hs * 8 + 2 * j + e], uv = ua[a][hs * 8 + 2 * j + e];
          const float sg = __builtin_amdgcn_rcpf(1.0f + __builtin_amdgcn_exp2f(gv * -1.4426950408889634f));
          r[e] = gv * sg * uv;
        }
        o[j] = pk_bf16(r[0], r[1]);
      }
      const int ks = nb * 8 + nw * 2 + hs;
      *(u32x4*)(ws + WS_H + ((size_t)mblk * 112 + ks) * 1024 + l * 16) = o;
    }
  }
}

constexpr int K2_LDS = GU_LDS > AT_LDS ? GU_LDS : AT_LDS;
__global__ __launch_bounds__(512, 4) void k2_mid(const u16* __restrict__ w1, const u16* __restrict__ w3, char* __restrict__ ws) {
  __shared__ __attribute__((aligned(16))) char smem[K2_LDS];
  const int b = blockIdx.x, x = b & 7, j = b >> 3, a = j >> 1;
  if (((j + (j >> 5)) & 1) == 0) {
    __builtin_amdgcn_s_setprio(2);
    attn_body(smem, ws, ws, x * 4 + (a & 3), a >> 2);
  } else if (a < 28) {
    gateup_body(smem, w1, w3, ws, (x >> 1) * 4 + a / 7, (x & 1) * 7 + a % 7);
  }
}

constexpr int FN_ST = 32768;
constexpr int FN_LDS = 3 * FN_ST;

__global__ __launch_bounds__(512, 2) void k3_final(const u16* __restrict__ x, const u16* __restrict__ w2, const u16* __restrict__ wo,
                                                   char* __restrict__ ws, u16* __restrict__ out) {
  __shared__ __attribute__((aligned(16))) char smem[FN_LDS];
  const int tid = threadIdx.x, l = tid & 63;
  const int w = __builtin_amdgcn_readfirstlane(tid >> 6);
  const int b = blockIdx.x, nb = (b >> 3) & 7, mb = (b & 7) * 4 + (b >> 6);
  const char* ah = ws + WS_H + ((size_t)(mb * 2 + (w >> 2)) * 112 + (w & 3) * 2) * 1024;
  const char* ao = ws + WS_O + ((size_t)(mb * 2 + (w >> 2)) * 32 + (w & 3) * 2) * 1024;
  const size_t brow = (size_t)(w * 16) * D + nb * 64;
  const unsigned bvo = (((l & 31) >> 2) * D + (l >> 5) * 32 + (l & 3) * 8) * 2;
  const unsigned sm = lds_addr(smem);
#define FN_STAGE(s, buf)                                                       \
  do {                                                                         \
    const char* a_ = (s) < 14 ? ah + (s) * 8192 : ao + ((s) - 14) * 8192;      \
    const u16* p_ = ((s) < 14 ? w2 + (size_t)(s) * 128 * D : wo + (size_t)((s) - 14) * 128 * D) + brow; \
    const unsigned d_ = sm + (buf) * FN_ST;                                    \
    glds16(a_, l * 16, d_ + (w >> 2) * 8192 + (w & 3) * 2048);                 \
    glds16(a_ + 1024, l * 16, d_ + (w >> 2) * 8192 + (w & 3) * 2048 + 1024);   \
    glds16(p_, bvo, d_ + 16384 + w * 2048);                                    \
    glds16(p_ + 8 * D, bvo, d_ + 16384 + w * 2048 + 1024);                     \
  } while (0)
  FN_STAGE(0, 0);
  FN_STAGE(1, 1);
  const int nh = w & 1, kq = w >> 1, h = l >> 5;
  const int aoff = kq * 2048 + l * 16;
  const int boff = 16384 + kq * 4096 + nh * 512 + (4 * h + ((l & 15) >> 2)) * 64 + ((l >> 4) & 1) * 32 + (l & 3) * 8;
  f32x16 acc[2] = {};
  int buf = 0;
  for (int s = 0; s < 18; s++) {
    if (s < 17) WAIT_V(4); else WAIT_V(0);
    BAR();
    if (s + 2 < 18) { const int nbuf = buf >= 1 ? buf - 1 : 2; FN_STAGE(s + 2, nbuf); }
    const char* sb = smem + buf * FN_ST;
#pragma unroll
    for (int ks = 0; ks < 2; ks++) {
      const bf16x8 a0 = lds_b128(sb + aoff + ks * 1024), a1 = lds_b128(sb + aoff + 8192 + ks * 1024);
      const char* bp = sb + boff + ks * 2048;
      const bf16x8 bw = lds_tr2(bp, bp + 1024);
      acc[0] = mfma32(bw, a0, acc[0]);
      acc[1] = mfma32(bw, a1, acc[1]);
    }
    buf = buf == 2 ? 0 : buf + 1;
  }
#undef FN_STAGE
  BAR();
#pragma unroll
  for (int a = 0; a < 2; a++)
#pragma unroll
    for (int j = 0; j < 4; j++) {
      f32x4 v = {acc[a][4 * j], acc[a][4 * j + 1], acc[a][4 * j + 2], acc[a][4 * j + 3]};
      *(LDSP(f32x4))(smem + ((kq * 64 + a * 32 + (l & 31)) * 68 + nh * 32 + 8 * j + 4 * h) * 4) = v;
    }
  BAR();
  const int tok = tid >> 3, n8 = (tid & 7) * 8;
  const size_t go = (size_t)(mb * 64 + tok) * D + nb * 64 + n8;
  const u32x4 xv = *(const u32x4*)(x + go);
  f32x4 s0 = {}, s1 = {};
#pragma unroll
  for (int q = 0; q < 4; q++) {
    const char* p = smem + ((q * 64 + tok) * 68 + n8) * 4;
    s0 += *(const LDSP(f32x4))p;
    s1 += *(const LDSP(f32x4))(p + 16);
  }
  u32x4 o;
  o[0] = pk_bf16(s0[0] + bf_lo(xv[0]), s0[1] + bf_hi(xv[0]));
  o[1] = pk_bf16(s0[2] + bf_lo(xv[1]), s0[3] + bf_hi(xv[1]));
  o[2] = pk_bf16(s1[0] + bf_lo(xv[2]), s1[1] + bf_hi(xv[2]));
  o[3] = pk_bf16(s1[2] + bf_lo(xv[3]), s1[3] + bf_hi(xv[3]));
  *(u32x4*)(out + go) = o;
}

extern "C" void kernel_launch(void* const* d_in, const int* in_sizes, int n_in, void* d_out, int out_size, void* d_ws,
                              size_t ws_size, hipStream_t stream) {
  const u16* x = (const u16*)d_in[0];
  const u16* wqkv = (const u16*)d_in[1];
  const u16* wo = (const u16*)d_in[2];
  const u16* w1 = (const u16*)d_in[4];
  const u16* w3 = (const u16*)d_in[5];
  const u16* w2 = (const u16*)d_in[6];
  char* ws = (char*)d_ws;
  k1_ln_qkv<<<256, 512, 0, stream>>>(x, wqkv, ws);
  k2_mid<<<512, 512, 0, stream>>>(w1, w3, ws);
  k3_final<<<256, 512, 0, stream>>>(x, w2, wo, ws, (u16*)d_out);
}
```

```cpp
#include <hip/hip_runtime.h>
#include <stdint.h>

typedef __bf16 bf16x2 __attribute__((ext_vector_type(2)));
typedef __bf16 bf16x4 __attribute__((ext_vector_type(4)));
typedef __bf16 bf16x8 __attribute__((ext_vector_type(8)));
typedef __bf16 bf16x16 __attribute__((ext_vector_type(16)));
typedef float f32x2 __attribute__((ext_vector_type(2)));
typedef float f32x4 __attribute__((ext_vector_type(4)));
typedef float f32x8 __attribute__((ext_vector_type(8)));
typedef float f32x16 __attribute__((ext_vector_type(16)));
typedef short s16x4 __attribute__((ext_vector_type(4)));
typedef unsigned u32x2 __attribute__((ext_vector_type(2)));
typedef unsigned u32x4 __attribute__((ext_vector_type(4)));
typedef unsigned short u16;

#define DEV __device__ __forceinline__
#define LDSP(T) __attribute__((address_space(3))) T*
#define WAIT_V(n) asm volatile("s_waitcnt vmcnt(" #n ")" ::: "memory")
#define WAIT_L(n) asm volatile("s_waitcnt lgkmcnt(" #n ")" ::: "memory")
#define BAR() do { asm volatile("s_waitcnt lgkmcnt(0)" ::: "memory"); __builtin_amdgcn_s_barrier(); asm volatile("" ::: "memory"); } while (0)
#define BARK() do { asm volatile("" ::: "memory"); __builtin_amdgcn_s_waitcnt(0xC07F); __builtin_amdgcn_s_barrier(); asm volatile("" ::: "memory"); } while (0)
#define SB() __builtin_amdgcn_sched_barrier(0)

constexpr int S = 2048, D = 512, NH = 32, F = 1792;
constexpr size_t WS_XN = 0;
constexpr size_t WS_Q = WS_XN + 2u * S * D;
constexpr size_t WS_K = WS_Q + 2u * S * D;
constexpr size_t WS_V = WS_K + 2u * S * D;
constexpr size_t WS_O = WS_V + 2u * S * D;
constexpr size_t WS_H = WS_O + 2u * S * D;

DEV void glds16(const void* base, unsigned voff, unsigned lds) {
  unsigned keep;
  asm volatile("s_mov_b32 %0, m0\n\ts_mov_b32 m0, %3\n\ts_nop 0\n\tglobal_load_lds_dwordx4 %1, %2\n\ts_mov_b32 m0, %0"
               : "=&s"(keep) : "v"(voff), "s"(base), "s"(lds) : "memory");
}
DEV void touch(const void* base, unsigned voff, unsigned lds) {
  unsigned keep;
  asm volatile("s_mov_b32 %0, m0\n\ts_mov_b32 m0, %3\n\ts_nop 0\n\tglobal_load_lds_dword %1, %2\n\ts_mov_b32 m0, %0"
               : "=&s"(keep) : "v"(voff), "s"(base), "s"(lds) : "memory");
}
DEV void st16(char* base, unsigned off, u32x4 v) {
  const __amdgpu_buffer_rsrc_t rs = __builtin_amdgcn_make_buffer_rsrc(base, 0, 0x7fffffff, 0x00020000);
  __builtin_amdgcn_raw_buffer_store_b128(v, rs, (int)off, 0, 16);
}
DEV unsigned lds_addr(const char* p) { return (unsigned)(uintptr_t)(const LDSP(char))p; }
DEV bf16x8 lds_b128(const char* p) { return *(const LDSP(bf16x8))p; }
DEV bf16x8 lds_tr2(const char* p0, const char* p1) {
  s16x4 a = __builtin_amdgcn_ds_read_tr16_b64_v4i16((LDSP(s16x4))p0);
  s16x4 b = __builtin_amdgcn_ds_read_tr16_b64_v4i16((LDSP(s16x4))p1);
  return __builtin_bit_cast(bf16x8, __builtin_shufflevector(a, b, 0, 1, 2, 3, 4, 5, 6, 7));
}
DEV unsigned pk_bf16(float a, float b) {
  f32x2 v = {a, b};
  return __builtin_bit_cast(unsigned, __builtin_convertvector(v, bf16x2));
}
DEV float bf_lo(unsigned u) { return __builtin_bit_cast(float, u << 16); }
DEV float bf_hi(unsigned u) { return __builtin_bit_cast(float, u & 0xffff0000u); }
DEV float sum2_bf16(unsigned u, float acc) {
  return __builtin_amdgcn_fdot2_f32_bf16(__builtin_bit_cast(bf16x2, u), __builtin_bit_cast(bf16x2, 0x3F803F80u), acc, false);
}
template <int CTRL> DEV float dpp_add(float v) {
  return v + __builtin_bit_cast(float, __builtin_amdgcn_update_dpp(0, __builtin_bit_cast(int, v), CTRL, 0xf, 0xf, true));
}
DEV float row16_sum(float v) {
  v = dpp_add<0xB1>(v);
  v = dpp_add<0x4E>(v);
  v = dpp_add<0x141>(v);
  v = dpp_add<0x140>(v);
  return v;
}
DEV f32x16 mfma32(bf16x8 a, bf16x8 b, f32x16 c) { return __builtin_amdgcn_mfma_f32_32x32x16_bf16(a, b, c, 0, 0, 0); }
DEV f32x4 mfma16(bf16x8 a, bf16x8 b, f32x4 c) { return __builtin_amdgcn_mfma_f32_16x16x32_bf16(a, b, c, 0, 0, 0); }

constexpr int K1_A = 65536;
constexpr int K1_K8 = 3104;
constexpr int K1_ST = 8 * K1_K8;
constexpr int K1_LDS = K1_A + 3 * K1_ST + 256;

__global__ __launch_bounds__(512, 2) void k1_ln_qkv(const u16* __restrict__ x, const u16* __restrict__ wqkv, char* __restrict__ ws,
                                                    const u16* __restrict__ w1, const u16* __restrict__ w3) {
  __shared__ __attribute__((aligned(16))) char smem[K1_LDS];
  const int tid = threadIdx.x, l = tid & 63;
  const int w = __builtin_amdgcn_readfirstlane(tid >> 6);
  const int b = blockIdx.x, mb = ((b >> 1) & 3) * 8 + ((b >> 3) & 7), nb = (b & 1) * 4 + (b >> 6);
  const int r4 = l >> 4, c = l & 15;
  u32x4 xr[2][4];
#pragma unroll
  for (int g = 0; g < 2; g++)
#pragma unroll
    for (int i = 0; i < 4; i++)
      xr[g][i] = *(const u32x4*)(x + (size_t)(mb * 64 + w * 8 + g * 4 + r4) * D + i * 128 + c * 8);
  const u16* wsrc = wqkv + (size_t)(w * 8) * (3 * D) + nb * 64;
  const unsigned wvo = (((l & 31) >> 2) * (3 * D) + (l >> 5) * 32 + (l & 3) * 8) * 2;
  const unsigned bst = lds_addr(smem) + K1_A + w * K1_K8;
#define K1_STAGE(s, buf)                                                       \
  do {                                                                         \
    const u16* p_ = wsrc + (size_t)(s) * 64 * (3 * D);                         \
    glds16(p_, wvo, bst + (buf) * K1_ST);                                      \
    glds16(p_ + D, wvo, bst + (buf) * K1_ST + 1024);                           \
    glds16(p_ + 2 * D, wvo, bst + (buf) * K1_ST + 2048);                       \
  } while (0)
#define K1_PIECE(s, buf, i) glds16(wsrc + (size_t)(s) * 64 * (3 * D) + (i) * D, wvo, bst + (buf) * K1_ST + (i) * 1024)
  const unsigned dummy = lds_addr(smem) + K1_LDS - 256;
  float rsum[2];
#pragma unroll
  for (int g = 0; g < 2; g++) {
    float s0 = 0.f, s1 = 0.f;
#pragma unroll
    for (int i = 0; i < 4; i++)
#pragma unroll
      for (int j = 0; j < 4; j += 2) {
        s0 = sum2_bf16(xr[g][i][j], s0);
        s1 = sum2_bf16(xr[g][i][j + 1], s1);
      }
    rsum[g] = s0 + s1;
  }
  asm volatile("" : "+v"(rsum[0]), "+v"(rsum[1]));
  K1_STAGE(0, 0);
  K1_STAGE(1, 1);
  {
    const int i = min(w * 64 + l, 287), r = i / 6, q = i % 6;
    touch(wqkv, (unsigned)(((128 + (mb & 7) * 48 + r) * (3 * D) + (q >> 1) * D + nb * 64 + (q & 1) * 32) * 2), dummy);
  }
#pragma unroll
  for (int g = 0; g < 2; g++) {
    const float m = row16_sum(rsum[g]) * (1.0f / D);
    f32x2 v[16], q2 = {0.f, 0.f};
#pragma unroll
    for (int j = 0; j < 16; j++) {
      v[j] = f32x2{bf_lo(xr[g][j >> 2][j & 3]), bf_hi(xr[g][j >> 2][j & 3])} - f32x2{m, m};
      q2 += v[j] * v[j];
    }
    const float rs = __builtin_amdgcn_rsqf(row16_sum(q2[0] + q2[1]) * (1.0f / D) + 1e-5f);
    const int row = w * 8 + g * 4 + r4, tok = row & 31;
    u32x4 keep = {};
#pragma unroll
    for (int i = 0; i < 4; i++) {
      u32x4 o;
#pragma unroll
      for (int j = 0; j < 4; j++) { const f32x2 t = v[i * 4 + j] * f32x2{rs, rs}; o[j] = pk_bf16(t[0], t[1]); }
      const int ks = 8 * i + (c >> 1), half = c & 1;
      *(LDSP(u32x4))(smem + (row >> 5) * 32768 + (ks * 64 + half * 32 + (tok ^ (c & 7))) * 16) = o;
      if (i == (nb >> 1)) keep = o;
    }
    if ((c >> 3) == (nb & 1))
      st16(ws + WS_XN, (((mb * 2 + (row >> 5)) * 32 + 8 * (nb >> 1) + (c >> 1)) * 64 + (c & 1) * 32 + tok) * 16, keep);
  }
  {
    touch(w < 4 ? w1 : w3, (unsigned)((b * 112 + min((w & 3) * 32 + (l & 31), 111)) * 64), dummy);
  }
  const int mw = w & 1, nw = w >> 1, g = l >> 4;
  const char* ab[2];
#pragma unroll
  for (int p = 0; p < 2; p++)
    ab[p] = smem + mw * 32768 + (((g >> 1) * 64 + (g & 1) * 32) + ((l & 15) ^ (g | (4 * p)))) * 16;
  const char* bb = smem + K1_A + g * K1_K8 + (nw >> 1) * 512 + ((l & 15) >> 2) * 64 + (nw & 1) * 32 + (l & 3) * 8;
  f32x4 aq[2] = {}, ak[2] = {}, av[2] = {};
  bf16x8 fa[2][4], fb[2][6];
#pragma unroll
  for (int s = 0; s <= 8; s++) {
    const int z = (s - 1) & 1, y = s & 1;
    const bool rd = s < 8, mm = s > 0, dma = s + 2 < 8;
    if (rd) {
      if (s < 2) WAIT_V(7); else if (s < 7) WAIT_V(3); else WAIT_V(0);
      BARK();
    }
#define K1_RA(p, i) if (rd) fa[y][2 * (p) + (i)] = lds_b128(ab[p] + (2 * s + (p)) * 2048 + (i) * 256)
#define K1_RB(p, i) if (rd) { const char* bp = bb + (s % 3) * K1_ST + (p) * 4 * K1_K8 + (i) * 1024; fb[y][3 * (p) + (i)] = lds_tr2(bp, bp + 256); }
#define K1_MQ(p, i) if (mm) aq[i] = mfma16(fb[z][3 * (p)], fa[z][2 * (p) + (i)], aq[i])
#define K1_MK(p, i) if (mm) ak[i] = mfma16(fb[z][3 * (p) + 1], fa[z][2 * (p) + (i)], ak[i])
#define K1_MV(p, i) if (mm) av[i] = mfma16(fa[z][2 * (p) + (i)], fb[z][3 * (p) + 2], av[i])
    if (dma) K1_PIECE(s + 2, (s + 2) % 3, 0);
    K1_RA(0, 0); K1_RA(0, 1); K1_MQ(0, 0); SB();
    if (dma) K1_PIECE(s + 2, (s + 2) % 3, 1);
    K1_RB(0, 0); K1_MQ(0, 1); SB();
    if (dma) K1_PIECE(s + 2, (s + 2) % 3, 2);
    K1_RB(0, 1); K1_MK(0, 0); SB();
    K1_RB(0, 2); K1_MK(0, 1); SB();
    K1_RA(1, 0); K1_RA(1, 1); K1_MV(0, 0); SB();
    K1_RB(1, 0); K1_MV(0, 1); SB();
    K1_RB(1, 1); K1_MQ(1, 0); SB();
    K1_RB(1, 2); K1_MQ(1, 1); SB();
    K1_MK(1, 0); K1_MK(1, 1); K1_MV(1, 0); K1_MV(1, 1); SB();
  }
#undef K1_PIECE
  const int head = nb * 4 + nw, tile = mb * 2 + mw;
  const size_t tb = ((size_t)head * 64 + tile) * 1024;
  const float qs = 0.25f * 1.4426950408889634f;
  {
    unsigned q[4], k[4], v[4];
#pragma unroll
    for (int e = 0; e < 2; e++) {
      auto rq = __builtin_amdgcn_permlane16_swap(pk_bf16(aq[0][2 * e] * qs, aq[0][2 * e + 1] * qs), pk_bf16(aq[1][2 * e] * qs, aq[1][2 * e + 1] * qs), false, false);
      auto rk = __builtin_amdgcn_permlane16_swap(pk_bf16(ak[0][2 * e], ak[0][2 * e + 1]), pk_bf16(ak[1][2 * e], ak[1][2 * e + 1]), false, false);
      auto rv = __builtin_amdgcn_permlane16_swap(pk_bf16(av[0][2 * e], av[0][2 * e + 1]), pk_bf16(av[1][2 * e], av[1][2 * e + 1]), false, false);
      q[e] = rq[0]; q[2 + e] = rq[1]; k[e] = rk[0]; k[2 + e] = rk[1]; v[e] = rv[0]; v[2 + e] = rv[1];
    }
    st16(ws + WS_Q + tb, l * 16, u32x4{q[0], q[1], q[2], q[3]});
    st16(ws + WS_K + tb, l * 16, u32x4{k[0], k[1], k[2], k[3]});
    st16(ws + WS_V + tb, (g & 1) * 512 + (g >> 1) * 256 + (l & 15) * 16, u32x4{v[0], v[1], v[2], v[3]});
  }
#undef K1_STAGE
}

constexpr int AT_CH = 8 * 1024 + 8 * 1152;
constexpr int AT_LDS = 3 * AT_CH;

DEV f32x16 pv_step(bf16x8 v, f32x8 p, f32x16 acc) {
  return __builtin_amdgcn_smfmac_f32_32x32x32_bf16(v, __builtin_bit_cast(bf16x16, p), acc, (int)0xDDDDDDDD, 0, 0);
}

DEV void attn_body(char* smem, const char* ws_c, char* ws, int head, int qb, const u16* w2, const u16* wo) {
  const int tid = threadIdx.x, l = tid & 63;
  const int w = __builtin_amdgcn_readfirstlane(tid >> 6);
  const char* kg = ws_c + WS_K + (size_t)head * 65536;
  const char* vg = ws_c + WS_V + (size_t)head * 65536;
  const unsigned sm = lds_addr(smem);
  const int qt = qb * 8 + w;
  const bf16x8 qf = *(const bf16x8*)(ws_c + WS_Q + ((size_t)head * 64 + qt) * 1024 + l * 16);
  if (tid < 3 * 8 * 8) {
    const int t = tid >> 3;
    const unsigned o1 = (tid & 3) == 0 ? 0x3F803F80u : 0u;
    *(LDSP(u32x4))(smem + (t >> 3) * AT_CH + 8192 + (t & 7) * 1152 + 1024 + (tid & 7) * 16) = u32x4{o1, o1, o1, o1};
  }
#define AT_STAGE(ch, buf)                                                      \
  do {                                                                         \
    glds16(kg + ((ch) * 8 + w) * 1024, l * 16, sm + (buf) * AT_CH + w * 1024); \
    glds16(vg + ((ch) * 8 + w) * 1024, l * 16, sm + (buf) * AT_CH + 8192 + w * 1152);\
  } while (0)
  AT_STAGE(0, 0);
  AT_STAGE(1, 1);
  const int d = l & 31, ha = l >> 5;
  const int cst = 8192 + 1024 + ((d >= 20 && d < 28) ? 64 : 0) + ((d == 16 || d == 20) ? 0 : 16);
  const int va0 = d < 16 ? 8192 + (ha * 16 + d) * 16 : cst;
  const int va1 = d < 16 ? va0 + 512 : cst;
  f32x16 acc = {};
  WAIT_V(2);
  BAR();
  {
    const int n64 = w < 4 ? 112 : 32;
    touch(w < 4 ? w2 : wo, (unsigned)(((head * 8 + qb) * n64 + min((w & 3) * 32 + (l & 31), n64 - 1)) * 64), sm + AT_LDS);
  }
  f32x16 sA = mfma32(lds_b128(smem + l * 16), qf, f32x16{});
  bf16x8 kn = lds_b128(smem + l * 16 + 1024);
  bf16x8 x0 = lds_b128(smem + va0), x1 = lds_b128(smem + va1);
  int buf = 0;
  for (int ch = 0; ch < 8; ch++) {
    const int nbuf = buf == 2 ? 0 : buf + 1;
    const char* cb = smem + buf * AT_CH;
    const char* cn = smem + (ch < 7 ? nbuf : buf) * AT_CH;
#pragma unroll
    for (int t = 0; t < 8; t++) {
      if (t == 6) {
        WAIT_V(0);
        BAR();
        if (ch + 2 < 8) { const int nb = buf >= 1 ? buf - 1 : 2; AT_STAGE(ch + 2, nb); }
      }
      f32x8 p0, p1;
#pragma unroll
      for (int j = 0; j < 4; j++) p0[j] = __builtin_amdgcn_exp2f(sA[j]);
      f32x16 sB = mfma32(kn, qf, f32x16{});
      kn = lds_b128((t + 2 < 8 ? cb + (t + 2) * 1024 : cn + (t + 2 - 8) * 1024) + l * 16);
#pragma unroll
      for (int j = 4; j < 8; j++) p0[j] = __builtin_amdgcn_exp2f(sA[j]);
      acc = pv_step(x0, p0, acc);
      x0 = lds_b128((t + 1 < 8 ? cb + (t + 1) * 1152 : cn) + va0);
#pragma unroll
      for (int j = 0; j < 8; j++) p1[j] = __builtin_amdgcn_exp2f(sA[8 + j]);
      acc = pv_step(x1, p1, acc);
      x1 = lds_b128((t + 1 < 8 ? cb + (t + 1) * 1152 : cn) + va1);
      sA = sB;
      __builtin_amdgcn_sched_group_barrier(0x400, 4, 0);
      __builtin_amdgcn_sched_group_barrier(0x008, 1, 0);
      __builtin_amdgcn_sched_group_barrier(0x100, 1, 0);
      __builtin_amdgcn_sched_group_barrier(0x400, 4, 0);
      __builtin_amdgcn_sched_group_barrier(0x008, 1, 0);
      __builtin_amdgcn_sched_group_barrier(0x100, 1, 0);
      __builtin_amdgcn_sched_group_barrier(0x400, 8, 0);
      __builtin_amdgcn_sched_group_barrier(0x008, 1, 0);
      __builtin_amdgcn_sched_group_barrier(0x100, 1, 0);
    }
    buf = nbuf;
  }
#undef AT_STAGE
  float lsum = acc[8];
  bool ok = lsum >= 0x1p-40f && lsum <= 0x1p40f;
#pragma unroll
  for (int j = 0; j < 8; j++) ok = ok && (__builtin_fabsf(acc[j]) < 0x1p120f);
  if (__builtin_amdgcn_ballot_w64(!ok) != 0) {
    float m = -__builtin_inff();
    acc = f32x16{};
    const bf16x8 ones = __builtin_bit_cast(bf16x8, u32x4{0x3F803F80u, 0x3F803F80u, 0x3F803F80u, 0x3F803F80u});
    const char* vl = ws_c + WS_V + (size_t)head * 65536 + (ha * 16 + (d & 15)) * 16;
    for (int t = 0; t < 64; t++) {
      const bf16x8 kf = *(const bf16x8*)(kg + t * 1024 + l * 16);
      bf16x8 x0 = *(const bf16x8*)(vl + t * 1024), x1 = *(const bf16x8*)(vl + t * 1024 + 512);
      if (d >= 16) { x0 = ones; x1 = ones; }
      f32x16 s = mfma32(kf, qf, f32x16{});
      float tm = s[0];
#pragma unroll
      for (int j = 1; j < 16; j++) tm = __builtin_fmaxf(tm, s[j]);
      tm = __builtin_fmaxf(tm, __shfl_xor(tm, 32));
      const float mn = __builtin_fmaxf(m, tm);
      const float al = __builtin_amdgcn_exp2f(m - mn);
      m = mn;
      f32x8 p0, p1;
#pragma unroll
      for (int j = 0; j < 8; j++) { p0[j] = __builtin_amdgcn_exp2f(s[j] - mn); p1[j] = __builtin_amdgcn_exp2f(s[8 + j] - mn); }
#pragma unroll
      for (int j = 0; j < 16; j++) acc[j] *= al;
      acc = pv_step(x0, p0, acc);
      acc = pv_step(x1, p1, acc);
    }
    lsum = acc[8];
  }
  const float inv = 1.0f / lsum;
  u32x4 o;
#pragma unroll
  for (int j = 0; j < 4; j++) o[j] = pk_bf16(acc[2 * j] * inv, acc[2 * j + 1] * inv);
  st16(ws + WS_O + ((size_t)qt * 32 + head) * 1024, l * 16, o);
}

constexpr int GU_ST = 8192 + 16384;
constexpr int GU_LDS = 3 * GU_ST;

DEV void gateup_body(char* smem, const u16* __restrict__ w1, const u16* __restrict__ w3, char* ws, int mb, int nb) {
  const int tid = threadIdx.x, l = tid & 63;
  const int w = __builtin_amdgcn_readfirstlane(tid >> 6);
  const char* ag = ws + WS_XN + ((size_t)(mb * 4 + (w >> 1)) * 32 + (w & 1)) * 1024;
  const unsigned sm = lds_addr(smem);
  const u16* bg = ((w & 1) ? w3 : w1) + (size_t)((w >> 1) * 8) * F + nb * 128;
  const unsigned bvo = (((l & 31) >> 2) * F + (l >> 5) * 32 + (l & 3) * 8) * 2;
#define GU_STAGE(s, buf)                                                       \
  do {                                                                         \
    glds16(ag + (s) * 2048, l * 16, sm + (buf) * GU_ST + w * 1024);            \
    const u16* p_ = bg + (size_t)(s) * 32 * F;                                 \
    glds16(p_, bvo, sm + (buf) * GU_ST + 8192 + w * 2048);                     \
    glds16(p_ + 64, bvo, sm + (buf) * GU_ST + 8192 + w * 2048 + 1024);         \
  } while (0)
  GU_STAGE(0, 0);
  GU_STAGE(1, 1);
  const int mw = w & 1, nw = w >> 1, h = l >> 5;
  const int aoff = mw * 4096 + l * 16;
  const int boff = 8192 + h * 4096 + nw * 512 + ((l & 15) >> 2) * 64 + ((l >> 4) & 1) * 32 + (l & 3) * 8;
  f32x16 ga[2] = {}, ua[2] = {};
  int buf = 0;
  for (int s = 0; s < 16; s++) {
    if (s < 15) WAIT_V(3); else WAIT_V(0);
    BAR();
    if (s + 2 < 16) { const int nbuf = buf >= 1 ? buf - 1 : 2; GU_STAGE(s + 2, nbuf); }
    const char* sb = smem + buf * GU_ST;
#pragma unroll
    for (int ks = 0; ks < 2; ks++) {
      const bf16x8 a0 = lds_b128(sb + aoff + ks * 1024), a1 = lds_b128(sb + aoff + 2048 + ks * 1024);
      const char* bp = sb + boff + ks * 8192;
      const bf16x8 bg_ = lds_tr2(bp, bp + 256), bu_ = lds_tr2(bp + 2048, bp + 2304);
      ga[0] = mfma32(bg_, a0, ga[0]); ua[0] = mfma32(bu_, a0, ua[0]);
      ga[1] = mfma32(bg_, a1, ga[1]); ua[1] = mfma32(bu_, a1, ua[1]);
    }
    buf = buf == 2 ? 0 : buf + 1;
  }
#undef GU_STAGE
#pragma unroll
  for (int a = 0; a < 2; a++) {
    const int mblk = mb * 4 + mw * 2 + a;
#pragma unroll
    for (int hs = 0; hs < 2; hs++) {
      u32x4 o;
#pragma unroll
      for (int j = 0; j < 4; j++) {
        float r[2];
#pragma unroll
        for (int e = 0; e < 2; e++) {
          const float gv = ga[a][hs * 8 + 2 * j + e], uv = ua[a][hs * 8 + 2 * j + e];
          const float sg = __builtin_amdgcn_rcpf(1.0f + __builtin_amdgcn_exp2f(gv * -1.4426950408889634f));
          r[e] = gv * sg * uv;
        }
        o[j] = pk_bf16(r[0], r[1]);
      }
      const int ks = nb * 8 + nw * 2 + hs;
      st16(ws + WS_H + ((size_t)mblk * 112 + ks) * 1024, l * 16, o);
    }
  }
}

constexpr int K2_LDS = GU_LDS > AT_LDS + 256 ? GU_LDS : AT_LDS + 256;
__global__ __launch_bounds__(512, 4) void k2_mid(const u16* __restrict__ w1, const u16* __restrict__ w3, char* __restrict__ ws,
                                                 const u16* __restrict__ w2, const u16* __restrict__ wo) {
  __shared__ __attribute__((aligned(16))) char smem[K2_LDS];
  const int b = blockIdx.x, x = b & 7, j = b >> 3, a = j >> 1;
  if (((j + (j >> 5)) & 1) == 0) {
    __builtin_amdgcn_s_setprio(2);
    attn_body(smem, ws, ws, x * 4 + (a & 3), a >> 2, w2, wo);
  } else if (a < 28) {
    gateup_body(smem, w1, w3, ws, (x >> 1) * 4 + a / 7, (x & 1) * 7 + a % 7);
  }
}

constexpr int FN_D = 5;
constexpr int FN_RS = 68;
constexpr int FN_LDS = 8 * 64 * FN_RS * 4;

DEV void gld16(bf16x8& dst, const void* base, unsigned voff) {
  asm volatile("global_load_dwordx4 %0, %1, %2" : "=&v"(dst) : "v"(voff), "s"(base) : "memory");
}

__global__ __launch_bounds__(512, 2) void k3_final(const u16* __restrict__ x, const u16* __restrict__ w2, const u16* __restrict__ wo,
                                                   char* __restrict__ ws, u16* __restrict__ out) {
  __shared__ __attribute__((aligned(16))) char smem[FN_LDS];
  const int tid = threadIdx.x, l = tid & 63;
  const int w = __builtin_amdgcn_readfirstlane(tid >> 6);
  const int b = blockIdx.x, nb = (b >> 3) & 7, mb = (b & 7) * 4 + (b >> 6);
  const char* ah = ws + WS_H + ((size_t)(mb * 2) * 112 + w) * 1024;
  const char* ao = ws + WS_O + ((size_t)(mb * 2) * 32 + w) * 1024;
  const size_t brow = (size_t)(w * 16) * D + nb * 64;
  const unsigned bvo = ((l >> 3) * D + ((l & 7) ^ (((l >> 4) & 1) << 2)) * 8) * 2;
  const unsigned ring = lds_addr(smem) + w * (FN_D * 2048);
  bf16x8 fa[FN_D + 1][2];
#define FN_ISSUE(i)                                                            \
  do {                                                                         \
    const u16* p_ = ((i) < 14 ? w2 + (size_t)(i) * 128 * D : wo + (size_t)((i) - 14) * 128 * D) + brow; \
    glds16(p_, bvo, ring + ((i) % FN_D) * 2048);                               \
    glds16(p_ + 8 * D, bvo, ring + ((i) % FN_D) * 2048 + 1024);                \
    gld16(fa[(i) % (FN_D + 1)][0], (i) < 14 ? ah + (i) * 8192 : ao + ((i) - 14) * 8192, l * 16); \
    gld16(fa[(i) % (FN_D + 1)][1], (i) < 14 ? ah + (i) * 8192 + 112 * 1024 : ao + ((i) - 14) * 8192 + 32 * 1024, l * 16); \
  } while (0)
  const int tok = tid >> 3, n8 = (tid & 7) * 8;
  const size_t go = (size_t)(mb * 64 + tok) * D + nb * 64 + n8;
  const u32x4 xv = *(const u32x4*)(x + go);
#pragma unroll
  for (int i = 0; i < FN_D - 1; i++) FN_ISSUE(i);
  const int h = l >> 5, rr = (l & 15) >> 2;
  const char* bb[2];
#pragma unroll
  for (int nh = 0; nh < 2; nh++)
    bb[nh] = smem + w * (FN_D * 2048) + (4 * h + rr) * 128 + (((nh * 4 + ((l >> 4) & 1) * 2 + ((l & 3) >> 1)) ^ ((rr >> 1) << 2)) * 16) + (l & 1) * 8;
  f32x16 acc[2][2] = {};
  bf16x8 fb[2][2];
#pragma unroll
  for (int i = 0; i <= 18; i++) {
    if (i < 18) {
      bf16x8 &a0 = fa[i % (FN_D + 1)][0], &a1 = fa[i % (FN_D + 1)][1];
      const int fl = (17 - i < FN_D - 2 ? 17 - i : FN_D - 2) * 4;
      if (fl == 12) asm volatile("s_waitcnt vmcnt(12)" : "+v"(a0), "+v"(a1) :: "memory");
      else if (fl == 8) asm volatile("s_waitcnt vmcnt(8)" : "+v"(a0), "+v"(a1) :: "memory");
      else if (fl == 4) asm volatile("s_waitcnt vmcnt(4)" : "+v"(a0), "+v"(a1) :: "memory");
      else asm volatile("s_waitcnt vmcnt(0)" : "+v"(a0), "+v"(a1) :: "memory");
#pragma unroll
      for (int nh = 0; nh < 2; nh++) fb[i & 1][nh] = lds_tr2(bb[nh] + (i % FN_D) * 2048, bb[nh] + (i % FN_D) * 2048 + 1024);
    }
    if (i > 0) {
      const int z = (i - 1) % (FN_D + 1), y = (i - 1) & 1;
      acc[0][0] = mfma32(fb[y][0], fa[z][0], acc[0][0]);
      acc[1][0] = mfma32(fb[y][0], fa[z][1], acc[1][0]);
      acc[0][1] = mfma32(fb[y][1], fa[z][0], acc[0][1]);
      acc[1][1] = mfma32(fb[y][1], fa[z][1], acc[1][1]);
    }
    SB();
    if (i + FN_D - 1 < 18) FN_ISSUE(i + FN_D - 1);
    SB();
  }
#undef FN_ISSUE
  BAR();
#pragma unroll
  for (int a = 0; a < 2; a++)
#pragma unroll
    for (int nh = 0; nh < 2; nh++)
#pragma unroll
      for (int j = 0; j < 4; j++) {
        f32x4 v = {acc[a][nh][4 * j], acc[a][nh][4 * j + 1], acc[a][nh][4 * j + 2], acc[a][nh][4 * j + 3]};
        *(LDSP(f32x4))(smem + ((w * 64 + a * 32 + (l & 31)) * FN_RS + nh * 32 + 8 * j + 4 * h) * 4) = v;
      }
  BAR();
  f32x4 s0 = {}, s1 = {};
#pragma unroll
  for (int q = 0; q < 8; q++) {
    const char* p = smem + ((q * 64 + tok) * FN_RS + n8) * 4;
    s0 += *(const LDSP(f32x4))p;
    s1 += *(const LDSP(f32x4))(p + 16);
  }
  u32x4 o;
  o[0] = pk_bf16(s0[0] + bf_lo(xv[0]), s0[1] + bf_hi(xv[0]));
  o[1] = pk_bf16(s0[2] + bf_lo(xv[1]), s0[3] + bf_hi(xv[1]));
  o[2] = pk_bf16(s1[0] + bf_lo(xv[2]), s1[1] + bf_hi(xv[2]));
  o[3] = pk_bf16(s1[2] + bf_lo(xv[3]), s1[3] + bf_hi(xv[3]));
  st16((char*)out, (unsigned)go * 2, o);
}

extern "C" void kernel_launch(void* const* d_in, const int* in_sizes, int n_in, void* d_out, int out_size, void* d_ws,
                              size_t ws_size, hipStream_t stream) {
  const u16* x = (const u16*)d_in[0];
  const u16* wqkv = (const u16*)d_in[1];
  const u16* wo = (const u16*)d_in[2];
  const u16* w1 = (const u16*)d_in[4];
  const u16* w3 = (const u16*)d_in[5];
  const u16* w2 = (const u16*)d_in[6];
  char* ws = (char*)d_ws;
  k1_ln_qkv<<<256, 512, 0, stream>>>(x, wqkv, ws, w1, w3);
  k2_mid<<<512, 512, 0, stream>>>(w1, w3, ws, w2, wo);
  k3_final<<<256, 512, 0, stream>>>(x, w2, wo, ws, (u16*)d_out);
}
```

```cpp
#include <hip/hip_runtime.h>
#include <stdint.h>

typedef __bf16 bf16x2 __attribute__((ext_vector_type(2)));
typedef __bf16 bf16x4 __attribute__((ext_vector_type(4)));
typedef __bf16 bf16x8 __attribute__((ext_vector_type(8)));
typedef __bf16 bf16x16 __attribute__((ext_vector_type(16)));
typedef float f32x2 __attribute__((ext_vector_type(2)));
typedef float f32x4 __attribute__((ext_vector_type(4)));
typedef float f32x8 __attribute__((ext_vector_type(8)));
typedef float f32x16 __attribute__((ext_vector_type(16)));
typedef short s16x4 __attribute__((ext_vector_type(4)));
typedef unsigned u32x2 __attribute__((ext_vector_type(2)));
typedef unsigned u32x4 __attribute__((ext_vector_type(4)));
typedef unsigned short u16;

#define DEV __device__ __forceinline__
#define LDSP(T) __attribute__((address_space(3))) T*
#define WAIT_V(n) asm volatile("s_waitcnt vmcnt(" #n ")" ::: "memory")
#define WAIT_L(n) asm volatile("s_waitcnt lgkmcnt(" #n ")" ::: "memory")
#define BAR() do { asm volatile("s_waitcnt lgkmcnt(0)" ::: "memory"); __builtin_amdgcn_s_barrier(); asm volatile("" ::: "memory"); } while (0)
#define BARK() do { asm volatile("" ::: "memory"); __builtin_amdgcn_s_waitcnt(0xC07F); __builtin_amdgcn_s_barrier(); asm volatile("" ::: "memory"); } while (0)
#define SB() __builtin_amdgcn_sched_barrier(0)

constexpr int S = 2048, D = 512, NH = 32, F = 1792;
constexpr size_t WS_XN = 0;
constexpr size_t WS_Q = WS_XN + 2u * S * D;
constexpr size_t WS_K = WS_Q + 2u * S * D;
constexpr size_t WS_V = WS_K + 2u * S * D;
constexpr size_t WS_O = WS_V + 2u * S * D;
constexpr size_t WS_H = WS_O + 2u * S * D;

DEV void glds16(const void* base, unsigned voff, unsigned lds) {
  unsigned keep;
  asm volatile("s_mov_b32 %0, m0\n\ts_mov_b32 m0, %3\n\ts_nop 0\n\tglobal_load_lds_dwordx4 %1, %2\n\ts_mov_b32 m0, %0"
               : "=&s"(keep) : "v"(voff), "s"(base), "s"(lds) : "memory");
}
DEV void touch(const void* base, unsigned voff, unsigned lds) {
  unsigned keep;
  asm volatile("s_mov_b32 %0, m0\n\ts_mov_b32 m0, %3\n\ts_nop 0\n\tglobal_load_lds_dword %1, %2\n\ts_mov_b32 m0, %0"
               : "=&s"(keep) : "v"(voff), "s"(base), "s"(lds) : "memory");
}
DEV void st16(char* base, unsigned off, u32x4 v) {
  const __amdgpu_buffer_rsrc_t rs = __builtin_amdgcn_make_buffer_rsrc(base, 0, 0x7fffffff, 0x00020000);
  __builtin_amdgcn_raw_buffer_store_b128(v, rs, (int)off, 0, 16);
}
DEV unsigned lds_addr(const char* p) { return (unsigned)(uintptr_t)(const LDSP(char))p; }
DEV bf16x8 lds_b128(const char* p) { return *(const LDSP(bf16x8))p; }
DEV bf16x8 lds_tr2(const char* p0, const char* p1) {
  s16x4 a = __builtin_amdgcn_ds_read_tr16_b64_v4i16((LDSP(s16x4))p0);
  s16x4 b = __builtin_amdgcn_ds_read_tr16_b64_v4i16((LDSP(s16x4))p1);
  return __builtin_bit_cast(bf16x8, __builtin_shufflevector(a, b, 0, 1, 2, 3, 4, 5, 6, 7));
}
DEV unsigned pk_bf16(float a, float b) {
  f32x2 v = {a, b};
  return __builtin_bit_cast(unsigned, __builtin_convertvector(v, bf16x2));
}
DEV float bf_lo(unsigned u) { return __builtin_bit_cast(float, u << 16); }
DEV float bf_hi(unsigned u) { return __builtin_bit_cast(float, u & 0xffff0000u); }
DEV float sum2_bf16(unsigned u, float acc) {
  return __builtin_amdgcn_fdot2_f32_bf16(__builtin_bit_cast(bf16x2, u), __builtin_bit_cast(bf16x2, 0x3F803F80u), acc, false);
}
template <int CTRL> DEV float dpp_add(float v) {
  return v + __builtin_bit_cast(float, __builtin_amdgcn_update_dpp(0, __builtin_bit_cast(int, v), CTRL, 0xf, 0xf, true));
}
DEV float row16_sum(float v) {
  v = dpp_add<0xB1>(v);
  v = dpp_add<0x4E>(v);
  v = dpp_add<0x141>(v);
  v = dpp_add<0x140>(v);
  return v;
}
DEV f32x16 mfma32(bf16x8 a, bf16x8 b, f32x16 c) { return __builtin_amdgcn_mfma_f32_32x32x16_bf16(a, b, c, 0, 0, 0); }
DEV f32x4 mfma16(bf16x8 a, bf16x8 b, f32x4 c) { return __builtin_amdgcn_mfma_f32_16x16x32_bf16(a, b, c, 0, 0, 0); }

constexpr int K1_A = 65536;
constexpr int K1_K8 = 3104;
constexpr int K1_ST = 8 * K1_K8;
constexpr int K1_LDS = K1_A + 3 * K1_ST + 256;

__global__ __launch_bounds__(512, 2) void k1_ln_qkv(const u16* __restrict__ x, const u16* __restrict__ wqkv, char* __restrict__ ws,
                                                    const u16* __restrict__ w1, const u16* __restrict__ w3) {
  __shared__ __attribute__((aligned(16))) char smem[K1_LDS];
  const int tid = threadIdx.x, l = tid & 63;
  const int w = __builtin_amdgcn_readfirstlane(tid >> 6);
  const int b = blockIdx.x, mb = ((b >> 1) & 3) * 8 + ((b >> 3) & 7), nb = (b & 1) * 4 + (b >> 6);
  const int r4 = l >> 4, c = l & 15;
  u32x4 xr[2][4];
#pragma unroll
  for (int g = 0; g < 2; g++)
#pragma unroll
    for (int i = 0; i < 4; i++)
      xr[g][i] = *(const u32x4*)(x + (size_t)(mb * 64 + w * 8 + g * 4 + r4) * D + i * 128 + c * 8);
  const u16* wsrc = wqkv + (size_t)(w * 8) * (3 * D) + nb * 64;
  const unsigned wvo = (((l & 31) >> 2) * (3 * D) + (l >> 5) * 32 + (l & 3) * 8) * 2;
  const unsigned bst = lds_addr(smem) + K1_A + w * K1_K8;
#define K1_STAGE(s, buf)                                                       \
  do {                                                                         \
    const u16* p_ = wsrc + (size_t)(s) * 64 * (3 * D);                         \
    glds16(p_, wvo, bst + (buf) * K1_ST);                                      \
    glds16(p_ + D, wvo, bst + (buf) * K1_ST + 1024);                           \
    glds16(p_ + 2 * D, wvo, bst + (buf) * K1_ST + 2048);                       \
  } while (0)
#define K1_PIECE(s, buf, i) glds16(wsrc + (size_t)(s) * 64 * (3 * D) + (i) * D, wvo, bst + (buf) * K1_ST + (i) * 1024)
  const unsigned dummy = lds_addr(smem) + K1_LDS - 256;
  float rsum[2];
#pragma unroll
  for (int g = 0; g < 2; g++) {
    float s0 = 0.f, s1 = 0.f;
#pragma unroll
    for (int i = 0; i < 4; i++)
#pragma unroll
      for (int j = 0; j < 4; j += 2) {
        s0 = sum2_bf16(xr[g][i][j], s0);
        s1 = sum2_bf16(xr[g][i][j + 1], s1);
      }
    rsum[g] = s0 + s1;
  }
  asm volatile("" : "+v"(rsum[0]), "+v"(rsum[1]));
  K1_STAGE(0, 0);
  K1_STAGE(1, 1);
  {
    const int i = min(w * 64 + l, 287), r = i / 6, q = i % 6;
    touch(wqkv, (unsigned)(((128 + (mb & 7) * 48 + r) * (3 * D) + (q >> 1) * D + nb * 64 + (q & 1) * 32) * 2), dummy);
  }
#pragma unroll
  for (int g = 0; g < 2; g++) {
    const float m = row16_sum(rsum[g]) * (1.0f / D);
    f32x2 v[16], q2 = {0.f, 0.f};
#pragma unroll
    for (int j = 0; j < 16; j++) {
      v[j] = f32x2{bf_lo(xr[g][j >> 2][j & 3]), bf_hi(xr[g][j >> 2][j & 3])} - f32x2{m, m};
      q2 += v[j] * v[j];
    }
    const float rs = __builtin_amdgcn_rsqf(row16_sum(q2[0] + q2[1]) * (1.0f / D) + 1e-5f);
    const int row = w * 8 + g * 4 + r4, tok = row & 31;
    u32x4 keep = {};
#pragma unroll
    for (int i = 0; i < 4; i++) {
      u32x4 o;
#pragma unroll
      for (int j = 0; j < 4; j++) { const f32x2 t = v[i * 4 + j] * f32x2{rs, rs}; o[j] = pk_bf16(t[0], t[1]); }
      const int ks = 8 * i + (c >> 1), half = c & 1;
      *(LDSP(u32x4))(smem + (row >> 5) * 32768 + (ks * 64 + half * 32 + (tok ^ (c & 7))) * 16) = o;
      if (i == (nb >> 1)) keep = o;
    }
    if ((c >> 3) == (nb & 1))
      st16(ws + WS_XN, (((mb * 2 + (row >> 5)) * 32 + 8 * (nb >> 1) + (c >> 1)) * 64 + (c & 1) * 32 + tok) * 16, keep);
  }
  {
    touch(w < 4 ? w1 : w3, (unsigned)((b * 112 + min((w & 3) * 32 + (l & 31), 111)) * 64), dummy);
  }
  const int mw = w & 1, nw = w >> 1, g = l >> 4;
  const char* ab[2];
#pragma unroll
  for (int p = 0; p < 2; p++)
    ab[p] = smem + mw * 32768 + (((g >> 1) * 64 + (g & 1) * 32) + ((l & 15) ^ (g | (4 * p)))) * 16;
  const char* bb = smem + K1_A + g * K1_K8 + (nw >> 1) * 512 + ((l & 15) >> 2) * 64 + (nw & 1) * 32 + (l & 3) * 8;
  f32x4 aq[2] = {}, ak[2] = {}, av[2] = {};
  bf16x8 fa[2][4], fb[2][6];
#pragma unroll
  for (int s = 0; s <= 8; s++) {
    const int z = (s - 1) & 1, y = s & 1;
    const bool rd = s < 8, mm = s > 0, dma = s + 2 < 8;
    if (rd) {
      if (s < 2) WAIT_V(7); else if (s < 7) WAIT_V(3); else WAIT_V(0);
      BARK();
    }
#define K1_RA(p, i) if (rd) fa[y][2 * (p) + (i)] = lds_b128(ab[p] + (2 * s + (p)) * 2048 + (i) * 256)
#define K1_RB(p, i) if (rd) { const char* bp = bb + (s % 3) * K1_ST + (p) * 4 * K1_K8 + (i) * 1024; fb[y][3 * (p) + (i)] = lds_tr2(bp, bp + 256); }
#define K1_MQ(p, i) if (mm) aq[i] = mfma16(fb[z][3 * (p)], fa[z][2 * (p) + (i)], aq[i])
#define K1_MK(p, i) if (mm) ak[i] = mfma16(fb[z][3 * (p) + 1], fa[z][2 * (p) + (i)], ak[i])
#define K1_MV(p, i) if (mm) av[i] = mfma16(fa[z][2 * (p) + (i)], fb[z][3 * (p) + 2], av[i])
    if (dma) K1_PIECE(s + 2, (s + 2) % 3, 0);
    K1_RA(0, 0); K1_RA(0, 1); K1_MQ(0, 0); SB();
    if (dma) K1_PIECE(s + 2, (s + 2) % 3, 1);
    K1_RB(0, 0); K1_MQ(0, 1); SB();
    if (dma) K1_PIECE(s + 2, (s + 2) % 3, 2);
    K1_RB(0, 1); K1_MK(0, 0); SB();
    K1_RB(0, 2); K1_MK(0, 1); SB();
    K1_RA(1, 0); K1_RA(1, 1); K1_MV(0, 0); SB();
    K1_RB(1, 0); K1_MV(0, 1); SB();
    K1_RB(1, 1); K1_MQ(1, 0); SB();
    K1_RB(1, 2); K1_MQ(1, 1); SB();
    K1_MK(1, 0); K1_MK(1, 1); K1_MV(1, 0); K1_MV(1, 1); SB();
  }
#undef K1_PIECE
  const int head = nb * 4 + nw, tile = mb * 2 + mw;
  const size_t tb = ((size_t)head * 64 + tile) * 1024;
  const float qs = 0.25f * 1.4426950408889634f;
  {
    unsigned q[4], k[4], v[4];
#pragma unroll
    for (int e = 0; e < 2; e++) {
      auto rq = __builtin_amdgcn_permlane16_swap(pk_bf16(aq[0][2 * e] * qs, aq[0][2 * e + 1] * qs), pk_bf16(aq[1][2 * e] * qs, aq[1][2 * e + 1] * qs), false, false);
      auto rk = __builtin_amdgcn_permlane16_swap(pk_bf16(ak[0][2 * e], ak[0][2 * e + 1]), pk_bf16(ak[1][2 * e], ak[1][2 * e + 1]), false, false);
      auto rv = __builtin_amdgcn_permlane16_swap(pk_bf16(av[0][2 * e], av[0][2 * e + 1]), pk_bf16(av[1][2 * e], av[1][2 * e + 1]), false, false);
      q[e] = rq[0]; q[2 + e] = rq[1]; k[e] = rk[0]; k[2 + e] = rk[1]; v[e] = rv[0]; v[2 + e] = rv[1];
    }
    st16(ws + WS_Q + tb, l * 16, u32x4{q[0], q[1], q[2], q[3]});
    st16(ws + WS_K + tb, l * 16, u32x4{k[0], k[1], k[2], k[3]});
    st16(ws + WS_V + tb, (g & 1) * 512 + (g >> 1) * 256 + (l & 15) * 16, u32x4{v[0], v[1], v[2], v[3]});
  }
#undef K1_STAGE
}

constexpr int AT_CH = 8 * 1024 + 8 * 1152;
constexpr int AT_LDS = 3 * AT_CH;

DEV f32x16 pv_step(bf16x8 v, f32x8 p, f32x16 acc) {
  return __builtin_amdgcn_smfmac_f32_32x32x32_bf16(v, __builtin_bit_cast(bf16x16, p), acc, (int)0xDDDDDDDD, 0, 0);
}

DEV void attn_body(char* smem, const char* ws_c, char* ws, int head, int qb, const u16* w2, const u16* wo) {
  const int tid = threadIdx.x, l = tid & 63;
  const int w = __builtin_amdgcn_readfirstlane(tid >> 6);
  const char* kg = ws_c + WS_K + (size_t)head * 65536;
  const char* vg = ws_c + WS_V + (size_t)head * 65536;
  const unsigned sm = lds_addr(smem);
  const int qt = qb * 8 + w;
  const bf16x8 qf = *(const bf16x8*)(ws_c + WS_Q + ((size_t)head * 64 + qt) * 1024 + l * 16);
  if (tid < 3 * 8 * 8) {
    const int t = tid >> 3;
    const unsigned o1 = (tid & 3) == 0 ? 0x3F803F80u : 0u;
    *(LDSP(u32x4))(smem + (t >> 3) * AT_CH + 8192 + (t & 7) * 1152 + 1024 + (tid & 7) * 16) = u32x4{o1, o1, o1, o1};
  }
#define AT_STAGE(ch, buf)                                                      \
  do {                                                                         \
    glds16(kg + ((ch) * 8 + w) * 1024, l * 16, sm + (buf) * AT_CH + w * 1024); \
    glds16(vg + ((ch) * 8 + w) * 1024, l * 16, sm + (buf) * AT_CH + 8192 + w * 1152);\
  } while (0)
  AT_STAGE(0, 0);
  AT_STAGE(1, 1);
  const int d = l & 31, ha = l >> 5;
  const int cst = 8192 + 1024 + ((d >= 20 && d < 28) ? 64 : 0) + ((d == 16 || d == 20) ? 0 : 16);
  const int va0 = d < 16 ? 8192 + (ha * 16 + d) * 16 : cst;
  const int va1 = d < 16 ? va0 + 512 : cst;
  f32x16 acc = {};
  WAIT_V(2);
  BAR();
  {
    const int n64 = w < 4 ? 112 : 32;
    touch(w < 4 ? w2 : wo, (unsigned)(((head * 8 + qb) * n64 + min((w & 3) * 32 + (l & 31), n64 - 1)) * 64), sm + AT_LDS);
  }
  f32x16 sA = mfma32(lds_b128(smem + l * 16), qf, f32x16{});
  bf16x8 kn = lds_b128(smem + l * 16 + 1024);
  bf16x8 x0 = lds_b128(smem + va0), x1 = lds_b128(smem + va1);
  int buf = 0;
  for (int ch = 0; ch < 8; ch++) {
    const int nbuf = buf == 2 ? 0 : buf + 1;
    const char* cb = smem + buf * AT_CH;
    const char* cn = smem + (ch < 7 ? nbuf : buf) * AT_CH;
#pragma unroll
    for (int t = 0; t < 8; t++) {
      if (t == 6) {
        WAIT_V(0);
        BAR();
        if (ch + 2 < 8) { const int nb = buf >= 1 ? buf - 1 : 2; AT_STAGE(ch + 2, nb); }
      }
      f32x8 p0, p1;
#pragma unroll
      for (int j = 0; j < 4; j++) p0[j] = __builtin_amdgcn_exp2f(sA[j]);
      f32x16 sB = mfma32(kn, qf, f32x16{});
      kn = lds_b128((t + 2 < 8 ? cb + (t + 2) * 1024 : cn + (t + 2 - 8) * 1024) + l * 16);
#pragma unroll
      for (int j = 4; j < 8; j++) p0[j] = __builtin_amdgcn_exp2f(sA[j]);
      acc = pv_step(x0, p0, acc);
      x0 = lds_b128((t + 1 < 8 ? cb + (t + 1) * 1152 : cn) + va0);
#pragma unroll
      for (int j = 0; j < 8; j++) p1[j] = __builtin_amdgcn_exp2f(sA[8 + j]);
      acc = pv_step(x1, p1, acc);
      x1 = lds_b128((t + 1 < 8 ? cb + (t + 1) * 1152 : cn) + va1);
      sA = sB;
      __builtin_amdgcn_sched_group_barrier(0x400, 4, 0);
      __builtin_amdgcn_sched_group_barrier(0x008, 1, 0);
      __builtin_amdgcn_sched_group_barrier(0x100, 1, 0);
      __builtin_amdgcn_sched_group_barrier(0x400, 4, 0);
      __builtin_amdgcn_sched_group_barrier(0x008, 1, 0);
      __builtin_amdgcn_sched_group_barrier(0x100, 1, 0);
      __builtin_amdgcn_sched_group_barrier(0x400, 8, 0);
      __builtin_amdgcn_sched_group_barrier(0x008, 1, 0);
      __builtin_amdgcn_sched_group_barrier(0x100, 1, 0);
    }
    buf = nbuf;
  }
#undef AT_STAGE
  float lsum = acc[8];
  bool ok = lsum >= 0x1p-40f && lsum <= 0x1p40f;
#pragma unroll
  for (int j = 0; j < 8; j++) ok = ok && (__builtin_fabsf(acc[j]) < 0x1p120f);
  if (__builtin_amdgcn_ballot_w64(!ok) != 0) {
    float m = -__builtin_inff();
    acc = f32x16{};
    const bf16x8 ones = __builtin_bit_cast(bf16x8, u32x4{0x3F803F80u, 0x3F803F80u, 0x3F803F80u, 0x3F803F80u});
    const char* vl = ws_c + WS_V + (size_t)head * 65536 + (ha * 16 + (d & 15)) * 16;
    for (int t = 0; t < 64; t++) {
      const bf16x8 kf = *(const bf16x8*)(kg + t * 1024 + l * 16);
      bf16x8 x0 = *(const bf16x8*)(vl + t * 1024), x1 = *(const bf16x8*)(vl + t * 1024 + 512);
      if (d >= 16) { x0 = ones; x1 = ones; }
      f32x16 s = mfma32(kf, qf, f32x16{});
      float tm = s[0];
#pragma unroll
      for (int j = 1; j < 16; j++) tm = __builtin_fmaxf(tm, s[j]);
      tm = __builtin_fmaxf(tm, __shfl_xor(tm, 32));
      const float mn = __builtin_fmaxf(m, tm);
      const float al = __builtin_amdgcn_exp2f(m - mn);
      m = mn;
      f32x8 p0, p1;
#pragma unroll
      for (int j = 0; j < 8; j++) { p0[j] = __builtin_amdgcn_exp2f(s[j] - mn); p1[j] = __builtin_amdgcn_exp2f(s[8 + j] - mn); }
#pragma unroll
      for (int j = 0; j < 16; j++) acc[j] *= al;
      acc = pv_step(x0, p0, acc);
      acc = pv_step(x1, p1, acc);
    }
    lsum = acc[8];
  }
  const float inv = 1.0f / lsum;
  u32x4 o;
#pragma unroll
  for (int j = 0; j < 4; j++) o[j] = pk_bf16(acc[2 * j] * inv, acc[2 * j + 1] * inv);
  st16(ws + WS_O + ((size_t)qt * 32 + head) * 1024, l * 16, o);
}

constexpr int GU_ST = 8192 + 16384;
constexpr int GU_LDS = 3 * GU_ST;

DEV void gateup_body(char* smem, const u16* __restrict__ w1, const u16* __restrict__ w3, char* ws, int mb, int nb) {
  const int tid = threadIdx.x, l = tid & 63;
  const int w = __builtin_amdgcn_readfirstlane(tid >> 6);
  const char* ag = ws + WS_XN + ((size_t)(mb * 4 + (w >> 1)) * 32 + (w & 1)) * 1024;
  const unsigned sm = lds_addr(smem);
  const u16* bg = ((w & 1) ? w3 : w1) + (size_t)((w >> 1) * 8) * F + nb * 128;
  const unsigned bvo = (((l & 31) >> 2) * F + (l >> 5) * 32 + (l & 3) * 8) * 2;
#define GU_STAGE(s, buf)                                                       \
  do {                                                                         \
    glds16(ag + (s) * 2048, l * 16, sm + (buf) * GU_ST + w * 1024);            \
    const u16* p_ = bg + (size_t)(s) * 32 * F;                                 \
    glds16(p_, bvo, sm + (buf) * GU_ST + 8192 + w * 2048);                     \
    glds16(p_ + 64, bvo, sm + (buf) * GU_ST + 8192 + w * 2048 + 1024);         \
  } while (0)
  GU_STAGE(0, 0);
  GU_STAGE(1, 1);
  const int mw = w & 1, nw = w >> 1, h = l >> 5;
  const int aoff = mw * 4096 + l * 16;
  const int boff = 8192 + h * 4096 + nw * 512 + ((l & 15) >> 2) * 64 + ((l >> 4) & 1) * 32 + (l & 3) * 8;
  f32x16 ga[2] = {}, ua[2] = {};
  int buf = 0;
  for (int s = 0; s < 16; s++) {
    if (s < 15) WAIT_V(3); else WAIT_V(0);
    BAR();
    if (s + 2 < 16) { const int nbuf = buf >= 1 ? buf - 1 : 2; GU_STAGE(s + 2, nbuf); }
    const char* sb = smem + buf * GU_ST;
#pragma unroll
    for (int ks = 0; ks < 2; ks++) {
      const bf16x8 a0 = lds_b128(sb + aoff + ks * 1024), a1 = lds_b128(sb + aoff + 2048 + ks * 1024);
      const char* bp = sb + boff + ks * 8192;
      const bf16x8 bg_ = lds_tr2(bp, bp + 256), bu_ = lds_tr2(bp + 2048, bp + 2304);
      ga[0] = mfma32(bg_, a0, ga[0]); ua[0] = mfma32(bu_, a0, ua[0]);
      ga[1] = mfma32(bg_, a1, ga[1]); ua[1] = mfma32(bu_, a1, ua[1]);
    }
    buf = buf == 2 ? 0 : buf + 1;
  }
#undef GU_STAGE
#pragma unroll
  for (int a = 0; a < 2; a++) {
    const int mblk = mb * 4 + mw * 2 + a;
#pragma unroll
    for (int hs = 0; hs < 2; hs++) {
      u32x4 o;
#pragma unroll
      for (int j = 0; j < 4; j++) {
        float r[2];
#pragma unroll
        for (int e = 0; e < 2; e++) {
          const float gv = ga[a][hs * 8 + 2 * j + e], uv = ua[a][hs * 8 + 2 * j + e];
          const float sg = __builtin_amdgcn_rcpf(1.0f + __builtin_amdgcn_exp2f(gv * -1.4426950408889634f));
          r[e] = gv * sg * uv;
        }
        o[j] = pk_bf16(r[0], r[1]);
      }
      const int ks = nb * 8 + nw * 2 + hs;
      st16(ws + WS_H + ((size_t)mblk * 112 + ks) * 1024, l * 16, o);
    }
  }
}

constexpr int K2_LDS = GU_LDS > AT_LDS + 256 ? GU_LDS : AT_LDS + 256;
__global__ __launch_bounds__(512, 4) void k2_mid(const u16* __restrict__ w1, const u16* __restrict__ w3, char* __restrict__ ws,
                                                 const u16* __restrict__ w2, const u16* __restrict__ wo) {
  __shared__ __attribute__((aligned(16))) char smem[K2_LDS];
  const int b = blockIdx.x, x = b & 7, j = b >> 3, a = j >> 1;
  if (((j + (j >> 5)) & 1) == 0) {
    __builtin_amdgcn_s_setprio(2);
    attn_body(smem, ws, ws, x * 4 + (a & 3), a >> 2, w2, wo);
  } else if (a < 28) {
    gateup_body(smem, w1, w3, ws, (x >> 1) * 4 + a / 7, (x & 1) * 7 + a % 7);
  }
}

constexpr int FN_ST = 32768;
constexpr int FN_LDS = 3 * FN_ST;

__global__ __launch_bounds__(512, 2) void k3_final(const u16* __restrict__ x, const u16* __restrict__ w2, const u16* __restrict__ wo,
                                                   char* __restrict__ ws, u16* __restrict__ out) {
  __shared__ __attribute__((aligned(16))) char smem[FN_LDS];
  const int tid = threadIdx.x, l = tid & 63;
  const int w = __builtin_amdgcn_readfirstlane(tid >> 6);
  const int b = blockIdx.x, nb = (b >> 3) & 7, mb = (b & 7) * 4 + (b >> 6);
  const char* ah = ws + WS_H + ((size_t)(mb * 2 + (w >> 2)) * 112 + (w & 3) * 2) * 1024;
  const char* ao = ws + WS_O + ((size_t)(mb * 2 + (w >> 2)) * 32 + (w & 3) * 2) * 1024;
  const size_t brow = (size_t)(w * 16) * D + nb * 64;
  const unsigned bvo = (((l & 31) >> 2) * D + (l >> 5) * 32 + (l & 3) * 8) * 2;
  const unsigned sm = lds_addr(smem);
#define FN_STAGE(s, buf)                                                       \
  do {                                                                         \
    const char* a_ = (s) < 14 ? ah + (s) * 8192 : ao + ((s) - 14) * 8192;      \
    const u16* p_ = ((s) < 14 ? w2 + (size_t)(s) * 128 * D : wo + (size_t)((s) - 14) * 128 * D) + brow; \
    const unsigned d_ = sm + (buf) * FN_ST;                                    \
    glds16(a_, l * 16, d_ + (w >> 2) * 8192 + (w & 3) * 2048);                 \
    glds16(a_ + 1024, l * 16, d_ + (w >> 2) * 8192 + (w & 3) * 2048 + 1024);   \
    glds16(p_, bvo, d_ + 16384 + w * 2048);                                    \
    glds16(p_ + 8 * D, bvo, d_ + 16384 + w * 2048 + 1024);                     \
  } while (0)
  FN_STAGE(0, 0);
  FN_STAGE(1, 1);
  const int nh = w & 1, kq = w >> 1, h = l >> 5;
  const int aoff = kq * 2048 + l * 16;
  const int boff = 16384 + kq * 4096 + nh * 512 + (4 * h + ((l & 15) >> 2)) * 64 + ((l >> 4) & 1) * 32 + (l & 3) * 8;
  f32x16 acc[2] = {};
  int buf = 0;
  for (int s = 0; s < 18; s++) {
    if (s < 17) WAIT_V(4); else WAIT_V(0);
    BAR();
    if (s + 2 < 18) { const int nbuf = buf >= 1 ? buf - 1 : 2; FN_STAGE(s + 2, nbuf); }
    const char* sb = smem + buf * FN_ST;
#pragma unroll
    for (int ks = 0; ks < 2; ks++) {
      const bf16x8 a0 = lds_b128(sb + aoff + ks * 1024), a1 = lds_b128(sb + aoff + 8192 + ks * 1024);
      const char* bp = sb + boff + ks * 2048;
      const bf16x8 bw = lds_tr2(bp, bp + 1024);
      acc[0] = mfma32(bw, a0, acc[0]);
      acc[1] = mfma32(bw, a1, acc[1]);
    }
    buf = buf == 2 ? 0 : buf + 1;
  }
#undef FN_STAGE
  BAR();
#pragma unroll
  for (int a = 0; a < 2; a++)
#pragma unroll
    for (int j = 0; j < 4; j++) {
      f32x4 v = {acc[a][4 * j], acc[a][4 * j + 1], acc[a][4 * j + 2], acc[a][4 * j + 3]};
      *(LDSP(f32x4))(smem + ((kq * 64 + a * 32 + (l & 31)) * 68 + nh * 32 + 8 * j + 4 * h) * 4) = v;
    }
  BAR();
  const int tok = tid >> 3, n8 = (tid & 7) * 8;
  const size_t go = (size_t)(mb * 64 + tok) * D + nb * 64 + n8;
  const u32x4 xv = *(const u32x4*)(x + go);
  f32x4 s0 = {}, s1 = {};
#pragma unroll
  for (int q = 0; q < 4; q++) {
    const char* p = smem + ((q * 64 + tok) * 68 + n8) * 4;
    s0 += *(const LDSP(f32x4))p;
    s1 += *(const LDSP(f32x4))(p + 16);
  }
  u32x4 o;
  o[0] = pk_bf16(s0[0] + bf_lo(xv[0]), s0[1] + bf_hi(xv[0]));
  o[1] = pk_bf16(s0[2] + bf_lo(xv[1]), s0[3] + bf_hi(xv[1]));
  o[2] = pk_bf16(s1[0] + bf_lo(xv[2]), s1[1] + bf_hi(xv[2]));
  o[3] = pk_bf16(s1[2] + bf_lo(xv[3]), s1[3] + bf_hi(xv[3]));
  *(u32x4*)(out + go) = o;
}

extern "C" void kernel_launch(void* const* d_in, const int* in_sizes, int n_in, void* d_out, int out_size, void* d_ws,
                              size_t ws_size, hipStream_t stream) {
  const u16* x = (const u16*)d_in[0];
  const u16* wqkv = (const u16*)d_in[1];
  const u16* wo = (const u16*)d_in[2];
  const u16* w1 = (const u16*)d_in[4];
  const u16* w3 = (const u16*)d_in[5];
  const u16* w2 = (const u16*)d_in[6];
  char* ws = (char*)d_ws;
  k1_ln_qkv<<<256, 512, 0, stream>>>(x, wqkv, ws, w1, w3);
  k2_mid<<<512, 512, 0, stream>>>(w1, w3, ws, w2, wo);
  k3_final<<<256, 512, 0, stream>>>(x, w2, wo, ws, (u16*)d_out);
}
```

```cpp
#define V_CODE
#include <hip/hip_runtime.h>
#include <stdint.h>

typedef __bf16 bf16x2 __attribute__((ext_vector_type(2)));
typedef __bf16 bf16x4 __attribute__((ext_vector_type(4)));
typedef __bf16 bf16x8 __attribute__((ext_vector_type(8)));
typedef __bf16 bf16x16 __attribute__((ext_vector_type(16)));
typedef float f32x2 __attribute__((ext_vector_type(2)));
typedef float f32x4 __attribute__((ext_vector_type(4)));
typedef float f32x8 __attribute__((ext_vector_type(8)));
typedef float f32x16 __attribute__((ext_vector_type(16)));
typedef short s16x4 __attribute__((ext_vector_type(4)));
typedef unsigned u32x2 __attribute__((ext_vector_type(2)));
typedef unsigned u32x4 __attribute__((ext_vector_type(4)));
typedef unsigned short u16;

#define DEV __device__ __forceinline__
#define LDSP(T) __attribute__((address_space(3))) T*
#define WAIT_V(n) asm volatile("s_waitcnt vmcnt(" #n ")" ::: "memory")
#define WAIT_L(n) asm volatile("s_waitcnt lgkmcnt(" #n ")" ::: "memory")
#define BAR() do { asm volatile("s_waitcnt lgkmcnt(0)" ::: "memory"); __builtin_amdgcn_s_barrier(); asm volatile("" ::: "memory"); } while (0)
#define BARK() do { asm volatile("" ::: "memory"); __builtin_amdgcn_s_waitcnt(0xC07F); __builtin_amdgcn_s_barrier(); asm volatile("" ::: "memory"); } while (0)
#define SB() __builtin_amdgcn_sched_barrier(0)

constexpr int S = 2048, D = 512, NH = 32, F = 1792;
constexpr size_t WS_XN = 0;
constexpr size_t WS_Q = WS_XN + 2u * S * D;
constexpr size_t WS_K = WS_Q + 2u * S * D;
constexpr size_t WS_V = WS_K + 2u * S * D;
constexpr size_t WS_O = WS_V + 2u * S * D;
constexpr size_t WS_H = WS_O + 2u * S * D;

DEV void glds16(const void* base, unsigned voff, unsigned lds) {
  unsigned keep;
  asm volatile("s_mov_b32 %0, m0\n\ts_mov_b32 m0, %3\n\ts_nop 0\n\tglobal_load_lds_dwordx4 %1, %2\n\ts_mov_b32 m0, %0"
               : "=&s"(keep) : "v"(voff), "s"(base), "s"(lds) : "memory");
}
DEV void touch(const void* base, unsigned voff, unsigned lds) {
  unsigned keep;
  asm volatile("s_mov_b32 %0, m0\n\ts_mov_b32 m0, %3\n\ts_nop 0\n\tglobal_load_lds_dword %1, %2\n\ts_mov_b32 m0, %0"
               : "=&s"(keep) : "v"(voff), "s"(base), "s"(lds) : "memory");
}
DEV void st16(char* base, unsigned off, u32x4 v) {
  const __amdgpu_buffer_rsrc_t rs = __builtin_amdgcn_make_buffer_rsrc(base, 0, 0x7fffffff, 0x00020000);
  __builtin_amdgcn_raw_buffer_store_b128(v, rs, (int)off, 0, 16);
}
DEV unsigned lds_addr(const char* p) { return (unsigned)(uintptr_t)(const LDSP(char))p; }
DEV bf16x8 lds_b128(const char* p) { return *(const LDSP(bf16x8))p; }
DEV bf16x8 lds_tr2(const char* p0, const char* p1) {
  s16x4 a = __builtin_amdgcn_ds_read_tr16_b64_v4i16((LDSP(s16x4))p0);
  s16x4 b = __builtin_amdgcn_ds_read_tr16_b64_v4i16((LDSP(s16x4))p1);
  return __builtin_bit_cast(bf16x8, __builtin_shufflevector(a, b, 0, 1, 2, 3, 4, 5, 6, 7));
}
DEV unsigned pk_bf16(float a, float b) {
  f32x2 v = {a, b};
  return __builtin_bit_cast(unsigned, __builtin_convertvector(v, bf16x2));
}
DEV float bf_lo(unsigned u) { return __builtin_bit_cast(float, u << 16); }
DEV float bf_hi(unsigned u) { return __builtin_bit_cast(float, u & 0xffff0000u); }
DEV float sum2_bf16(unsigned u, float acc) {
  return __builtin_amdgcn_fdot2_f32_bf16(__builtin_bit_cast(bf16x2, u), __builtin_bit_cast(bf16x2, 0x3F803F80u), acc, false);
}
template <int CTRL> DEV float dpp_add(float v) {
  return v + __builtin_bit_cast(float, __builtin_amdgcn_update_dpp(0, __builtin_bit_cast(int, v), CTRL, 0xf, 0xf, true));
}
DEV float row16_sum(float v) {
  v = dpp_add<0xB1>(v);
  v = dpp_add<0x4E>(v);
  v = dpp_add<0x141>(v);
  v = dpp_add<0x140>(v);
  return v;
}
DEV f32x16 mfma32(bf16x8 a, bf16x8 b, f32x16 c) { return __builtin_amdgcn_mfma_f32_32x32x16_bf16(a, b, c, 0, 0, 0); }
DEV f32x4 mfma16(bf16x8 a, bf16x8 b, f32x4 c) { return __builtin_amdgcn_mfma_f32_16x16x32_bf16(a, b, c, 0, 0, 0); }

constexpr int K1_A = 65536;
constexpr int K1_K8 = 3104;
constexpr int K1_ST = 8 * K1_K8;
constexpr int K1_LDS = K1_A + 3 * K1_ST + 256;

__global__ __launch_bounds__(512, 2) void k1_ln_qkv(const u16* __restrict__ x, const u16* __restrict__ wqkv, char* __restrict__ ws,
                                                    const u16* __restrict__ w1, const u16* __restrict__ w3, const char* outp) {
  __shared__ __attribute__((aligned(16))) char smem[K1_LDS];
  const int tid = threadIdx.x, l = tid & 63;
  const int w = __builtin_amdgcn_readfirstlane(tid >> 6);
  const int b = blockIdx.x, mb = ((b >> 1) & 3) * 8 + ((b >> 3) & 7), nb = (b & 1) * 4 + (b >> 6);
  const int r4 = l >> 4, c = l & 15;
  u32x4 xr[2][4];
#pragma unroll
  for (int g = 0; g < 2; g++)
#pragma unroll
    for (int i = 0; i < 4; i++)
      xr[g][i] = *(const u32x4*)(x + (size_t)(mb * 64 + w * 8 + g * 4 + r4) * D + i * 128 + c * 8);
  const u16* wsrc = wqkv + (size_t)(w * 8) * (3 * D) + nb * 64;
  const unsigned wvo = (((l & 31) >> 2) * (3 * D) + (l >> 5) * 32 + (l & 3) * 8) * 2;
  const unsigned bst = lds_addr(smem) + K1_A + w * K1_K8;
#define K1_STAGE(s, buf)                                                       \
  do {                                                                         \
    const u16* p_ = wsrc + (size_t)(s) * 64 * (3 * D);                         \
    glds16(p_, wvo, bst + (buf) * K1_ST);                                      \
    glds16(p_ + D, wvo, bst + (buf) * K1_ST + 1024);                           \
    glds16(p_ + 2 * D, wvo, bst + (buf) * K1_ST + 2048);                       \
  } while (0)
#define K1_PIECE(s, buf, i) glds16(wsrc + (size_t)(s) * 64 * (3 * D) + (i) * D, wvo, bst + (buf) * K1_ST + (i) * 1024)
  const unsigned dummy = lds_addr(smem) + K1_LDS - 256;
  float rsum[2];
#pragma unroll
  for (int g = 0; g < 2; g++) {
    float s0 = 0.f, s1 = 0.f;
#pragma unroll
    for (int i = 0; i < 4; i++)
#pragma unroll
      for (int j = 0; j < 4; j += 2) {
        s0 = sum2_bf16(xr[g][i][j], s0);
        s1 = sum2_bf16(xr[g][i][j + 1], s1);
      }
    rsum[g] = s0 + s1;
  }
  asm volatile("" : "+v"(rsum[0]), "+v"(rsum[1]));
  K1_STAGE(0, 0);
  K1_STAGE(1, 1);
  {
    const int i = min(w * 64 + l, 287), r = i / 6, q = i % 6;
    touch(wqkv, (unsigned)(((128 + (mb & 7) * 48 + r) * (3 * D) + (q >> 1) * D + nb * 64 + (q & 1) * 32) * 2), dummy);
  }
#pragma unroll
  for (int g = 0; g < 2; g++) {
    const float m = row16_sum(rsum[g]) * (1.0f / D);
    f32x2 v[16], q2 = {0.f, 0.f};
#pragma unroll
    for (int j = 0; j < 16; j++) {
      v[j] = f32x2{bf_lo(xr[g][j >> 2][j & 3]), bf_hi(xr[g][j >> 2][j & 3])} - f32x2{m, m};
      q2 += v[j] * v[j];
    }
    const float rs = __builtin_amdgcn_rsqf(row16_sum(q2[0] + q2[1]) * (1.0f / D) + 1e-5f);
    const int row = w * 8 + g * 4 + r4, tok = row & 31;
    u32x4 keep = {};
#pragma unroll
    for (int i = 0; i < 4; i++) {
      u32x4 o;
#pragma unroll
      for (int j = 0; j < 4; j++) { const f32x2 t = v[i * 4 + j] * f32x2{rs, rs}; o[j] = pk_bf16(t[0], t[1]); }
      const int ks = 8 * i + (c >> 1), half = c & 1;
      *(LDSP(u32x4))(smem + (row >> 5) * 32768 + (ks * 64 + half * 32 + (tok ^ (c & 7))) * 16) = o;
      if (i == (nb >> 1)) keep = o;
    }
    if ((c >> 3) == (nb & 1))
      st16(ws + WS_XN, (((mb * 2 + (row >> 5)) * 32 + 8 * (nb >> 1) + (c >> 1)) * 64 + (c & 1) * 32 + tok) * 16, keep);
  }
  {
    touch(w < 4 ? w1 : w3, (unsigned)((b * 112 + min((w & 3) * 32 + (l & 31), 111)) * 64), dummy);
  }
  if (w == 6) {
    unsigned long long pc; int o2, o3;
    asm volatile("s_getpc_b64 %0\n1:\n\ts_mov_b32 %1, _Z6k2_midPKtS0_PcS0_S0_-1b\n\ts_mov_b32 %2, _Z8k3_finalPKtS0_S0_PcPt-1b" : "=s"(pc), "=s"(o2), "=s"(o3));
    const int j = b >> 3;
    const char* base = (const char*)(pc + (long long)o2);
    touch(base, (unsigned)((j * 8 + (l & 7)) * 64), dummy);
  }
  const int mw = w & 1, nw = w >> 1, g = l >> 4;
  const char* ab[2];
#pragma unroll
  for (int p = 0; p < 2; p++)
    ab[p] = smem + mw * 32768 + (((g >> 1) * 64 + (g & 1) * 32) + ((l & 15) ^ (g | (4 * p)))) * 16;
  const char* bb = smem + K1_A + g * K1_K8 + (nw >> 1) * 512 + ((l & 15) >> 2) * 64 + (nw & 1) * 32 + (l & 3) * 8;
  f32x4 aq[2] = {}, ak[2] = {}, av[2] = {};
  bf16x8 fa[2][4], fb[2][6];
#pragma unroll
  for (int s = 0; s <= 8; s++) {
    const int z = (s - 1) & 1, y = s & 1;
    const bool rd = s < 8, mm = s > 0, dma = s + 2 < 8;
    if (rd) {
      if (s < 2) WAIT_V(7); else if (s < 7) WAIT_V(3); else WAIT_V(0);
      BARK();
    }
#define K1_RA(p, i) if (rd) fa[y][2 * (p) + (i)] = lds_b128(ab[p] + (2 * s + (p)) * 2048 + (i) * 256)
#define K1_RB(p, i) if (rd) { const char* bp = bb + (s % 3) * K1_ST + (p) * 4 * K1_K8 + (i) * 1024; fb[y][3 * (p) + (i)] = lds_tr2(bp, bp + 256); }
#define K1_MQ(p, i) if (mm) aq[i] = mfma16(fb[z][3 * (p)], fa[z][2 * (p) + (i)], aq[i])
#define K1_MK(p, i) if (mm) ak[i] = mfma16(fb[z][3 * (p) + 1], fa[z][2 * (p) + (i)], ak[i])
#define K1_MV(p, i) if (mm) av[i] = mfma16(fa[z][2 * (p) + (i)], fb[z][3 * (p) + 2], av[i])
    if (dma) K1_PIECE(s + 2, (s + 2) % 3, 0);
    K1_RA(0, 0); K1_RA(0, 1); K1_MQ(0, 0); SB();
    if (dma) K1_PIECE(s + 2, (s + 2) % 3, 1);
    K1_RB(0, 0); K1_MQ(0, 1); SB();
    if (dma) K1_PIECE(s + 2, (s + 2) % 3, 2);
    K1_RB(0, 1); K1_MK(0, 0); SB();
    K1_RB(0, 2); K1_MK(0, 1); SB();
    K1_RA(1, 0); K1_RA(1, 1); K1_MV(0, 0); SB();
    K1_RB(1, 0); K1_MV(0, 1); SB();
    K1_RB(1, 1); K1_MQ(1, 0); SB();
    K1_RB(1, 2); K1_MQ(1, 1); SB();
    K1_MK(1, 0); K1_MK(1, 1); K1_MV(1, 0); K1_MV(1, 1); SB();
  }
#undef K1_PIECE
  const int head = nb * 4 + nw, tile = mb * 2 + mw;
  const size_t tb = ((size_t)head * 64 + tile) * 1024;
  const float qs = 0.25f * 1.4426950408889634f;
  {
    unsigned q[4], k[4], v[4];
#pragma unroll
    for (int e = 0; e < 2; e++) {
      auto rq = __builtin_amdgcn_permlane16_swap(pk_bf16(aq[0][2 * e] * qs, aq[0][2 * e + 1] * qs), pk_bf16(aq[1][2 * e] * qs, aq[1][2 * e + 1] * qs), false, false);
      auto rk = __builtin_amdgcn_permlane16_swap(pk_bf16(ak[0][2 * e], ak[0][2 * e + 1]), pk_bf16(ak[1][2 * e], ak[1][2 * e + 1]), false, false);
      auto rv = __builtin_amdgcn_permlane16_swap(pk_bf16(av[0][2 * e], av[0][2 * e + 1]), pk_bf16(av[1][2 * e], av[1][2 * e + 1]), false, false);
      q[e] = rq[0]; q[2 + e] = rq[1]; k[e] = rk[0]; k[2 + e] = rk[1]; v[e] = rv[0]; v[2 + e] = rv[1];
    }
    st16(ws + WS_Q + tb, l * 16, u32x4{q[0], q[1], q[2], q[3]});
    st16(ws + WS_K + tb, l * 16, u32x4{k[0], k[1], k[2], k[3]});
    st16(ws + WS_V + tb, (g & 1) * 512 + (g >> 1) * 256 + (l & 15) * 16, u32x4{v[0], v[1], v[2], v[3]});
  }
#undef K1_STAGE
}

constexpr int AT_CH = 8 * 1024 + 8 * 1152;
constexpr int AT_LDS = 3 * AT_CH;

DEV f32x16 pv_step(bf16x8 v, f32x8 p, f32x16 acc) {
  return __builtin_amdgcn_smfmac_f32_32x32x32_bf16(v, __builtin_bit_cast(bf16x16, p), acc, (int)0xDDDDDDDD, 0, 0);
}

DEV void attn_body(char* smem, const char* ws_c, char* ws, int head, int qb, const u16* w2, const u16* wo) {
  const int tid = threadIdx.x, l = tid & 63;
  const int w = __builtin_amdgcn_readfirstlane(tid >> 6);
  const char* kg = ws_c + WS_K + (size_t)head * 65536;
  const char* vg = ws_c + WS_V + (size_t)head * 65536;
  const unsigned sm = lds_addr(smem);
  const int qt = qb * 8 + w;
  const bf16x8 qf = *(const bf16x8*)(ws_c + WS_Q + ((size_t)head * 64 + qt) * 1024 + l * 16);
  if (tid < 3 * 8 * 8) {
    const int t = tid >> 3;
    const unsigned o1 = (tid & 3) == 0 ? 0x3F803F80u : 0u;
    *(LDSP(u32x4))(smem + (t >> 3) * AT_CH + 8192 + (t & 7) * 1152 + 1024 + (tid & 7) * 16) = u32x4{o1, o1, o1, o1};
  }
#define AT_STAGE(ch, buf)                                                      \
  do {                                                                         \
    glds16(kg + ((ch) * 8 + w) * 1024, l * 16, sm + (buf) * AT_CH + w * 1024); \
    glds16(vg + ((ch) * 8 + w) * 1024, l * 16, sm + (buf) * AT_CH + 8192 + w * 1152);\
  } while (0)
  AT_STAGE(0, 0);
  AT_STAGE(1, 1);
  const int d = l & 31, ha = l >> 5;
  const int cst = 8192 + 1024 + ((d >= 20 && d < 28) ? 64 : 0) + ((d == 16 || d == 20) ? 0 : 16);
  const int va0 = d < 16 ? 8192 + (ha * 16 + d) * 16 : cst;
  const int va1 = d < 16 ? va0 + 512 : cst;
  f32x16 acc = {};
  WAIT_V(2);
  BAR();
  {
    const int n64 = w < 4 ? 112 : 32;
    touch(w < 4 ? w2 : wo, (unsigned)(((head * 8 + qb) * n64 + min((w & 3) * 32 + (l & 31), n64 - 1)) * 64), sm + AT_LDS);
  }
  f32x16 sA = mfma32(lds_b128(smem + l * 16), qf, f32x16{});
  bf16x8 kn = lds_b128(smem + l * 16 + 1024);
  bf16x8 x0 = lds_b128(smem + va0), x1 = lds_b128(smem + va1);
  int buf = 0;
  for (int ch = 0; ch < 8; ch++) {
    const int nbuf = buf == 2 ? 0 : buf + 1;
    const char* cb = smem + buf * AT_CH;
    const char* cn = smem + (ch < 7 ? nbuf : buf) * AT_CH;
#pragma unroll
    for (int t = 0; t < 8; t++) {
      if (t == 6) {
        WAIT_V(0);
        BAR();
        if (ch + 2 < 8) { const int nb = buf >= 1 ? buf - 1 : 2; AT_STAGE(ch + 2, nb); }
      }
      f32x8 p0, p1;
#pragma unroll
      for (int j = 0; j < 4; j++) p0[j] = __builtin_amdgcn_exp2f(sA[j]);
      f32x16 sB = mfma32(kn, qf, f32x16{});
      kn = lds_b128((t + 2 < 8 ? cb + (t + 2) * 1024 : cn + (t + 2 - 8) * 1024) + l * 16);
#pragma unroll
      for (int j = 4; j < 8; j++) p0[j] = __builtin_amdgcn_exp2f(sA[j]);
      acc = pv_step(x0, p0, acc);
      x0 = lds_b128((t + 1 < 8 ? cb + (t + 1) * 1152 : cn) + va0);
#pragma unroll
      for (int j = 0; j < 8; j++) p1[j] = __builtin_amdgcn_exp2f(sA[8 + j]);
      acc = pv_step(x1, p1, acc);
      x1 = lds_b128((t + 1 < 8 ? cb + (t + 1) * 1152 : cn) + va1);
      sA = sB;
      __builtin_amdgcn_sched_group_barrier(0x400, 4, 0);
      __builtin_amdgcn_sched_group_barrier(0x008, 1, 0);
      __builtin_amdgcn_sched_group_barrier(0x100, 1, 0);
      __builtin_amdgcn_sched_group_barrier(0x400, 4, 0);
      __builtin_amdgcn_sched_group_barrier(0x008, 1, 0);
      __builtin_amdgcn_sched_group_barrier(0x100, 1, 0);
      __builtin_amdgcn_sched_group_barrier(0x400, 8, 0);
      __builtin_amdgcn_sched_group_barrier(0x008, 1, 0);
      __builtin_amdgcn_sched_group_barrier(0x100, 1, 0);
    }
    buf = nbuf;
  }
#undef AT_STAGE
  float lsum = acc[8];
  bool ok = lsum >= 0x1p-40f && lsum <= 0x1p40f;
#pragma unroll
  for (int j = 0; j < 8; j++) ok = ok && (__builtin_fabsf(acc[j]) < 0x1p120f);
  if (__builtin_amdgcn_ballot_w64(!ok) != 0) {
    float m = -__builtin_inff();
    acc = f32x16{};
    const bf16x8 ones = __builtin_bit_cast(bf16x8, u32x4{0x3F803F80u, 0x3F803F80u, 0x3F803F80u, 0x3F803F80u});
    const char* vl = ws_c + WS_V + (size_t)head * 65536 + (ha * 16 + (d & 15)) * 16;
    for (int t = 0; t < 64; t++) {
      const bf16x8 kf = *(const bf16x8*)(kg + t * 1024 + l * 16);
      bf16x8 x0 = *(const bf16x8*)(vl + t * 1024), x1 = *(const bf16x8*)(vl + t * 1024 + 512);
      if (d >= 16) { x0 = ones; x1 = ones; }
      f32x16 s = mfma32(kf, qf, f32x16{});
      float tm = s[0];
#pragma unroll
      for (int j = 1; j < 16; j++) tm = __builtin_fmaxf(tm, s[j]);
      tm = __builtin_fmaxf(tm, __shfl_xor(tm, 32));
      const float mn = __builtin_fmaxf(m, tm);
      const float al = __builtin_amdgcn_exp2f(m - mn);
      m = mn;
      f32x8 p0, p1;
#pragma unroll
      for (int j = 0; j < 8; j++) { p0[j] = __builtin_amdgcn_exp2f(s[j] - mn); p1[j] = __builtin_amdgcn_exp2f(s[8 + j] - mn); }
#pragma unroll
      for (int j = 0; j < 16; j++) acc[j] *= al;
      acc = pv_step(x0, p0, acc);
      acc = pv_step(x1, p1, acc);
    }
    lsum = acc[8];
  }
  const float inv = 1.0f / lsum;
  u32x4 o;
#pragma unroll
  for (int j = 0; j < 4; j++) o[j] = pk_bf16(acc[2 * j] * inv, acc[2 * j + 1] * inv);
  st16(ws + WS_O + ((size_t)qt * 32 + head) * 1024, l * 16, o);
}

constexpr int GU_ST = 8192 + 16384;
constexpr int GU_LDS = 3 * GU_ST;

DEV void gateup_body(char* smem, const u16* __restrict__ w1, const u16* __restrict__ w3, char* ws, int mb, int nb) {
  const int tid = threadIdx.x, l = tid & 63;
  const int w = __builtin_amdgcn_readfirstlane(tid >> 6);
  const char* ag = ws + WS_XN + ((size_t)(mb * 4 + (w >> 1)) * 32 + (w & 1)) * 1024;
  const unsigned sm = lds_addr(smem);
  const u16* bg = ((w & 1) ? w3 : w1) + (size_t)((w >> 1) * 8) * F + nb * 128;
  const unsigned bvo = (((l & 31) >> 2) * F + (l >> 5) * 32 + (l & 3) * 8) * 2;
#define GU_STAGE(s, buf)                                                       \
  do {                                                                         \
    glds16(ag + (s) * 2048, l * 16, sm + (buf) * GU_ST + w * 1024);            \
    const u16* p_ = bg + (size_t)(s) * 32 * F;                                 \
    glds16(p_, bvo, sm + (buf) * GU_ST + 8192 + w * 2048);                     \
    glds16(p_ + 64, bvo, sm + (buf) * GU_ST + 8192 + w * 2048 + 1024);         \
  } while (0)
  GU_STAGE(0, 0);
  GU_STAGE(1, 1);
  const int mw = w & 1, nw = w >> 1, h = l >> 5;
  const int aoff = mw * 4096 + l * 16;
  const int boff = 8192 + h * 4096 + nw * 512 + ((l & 15) >> 2) * 64 + ((l >> 4) & 1) * 32 + (l & 3) * 8;
  f32x16 ga[2] = {}, ua[2] = {};
  int buf = 0;
  for (int s = 0; s < 16; s++) {
    if (s < 15) WAIT_V(3); else WAIT_V(0);
    BAR();
    if (s + 2 < 16) { const int nbuf = buf >= 1 ? buf - 1 : 2; GU_STAGE(s + 2, nbuf); }
    const char* sb = smem + buf * GU_ST;
#pragma unroll
    for (int ks = 0; ks < 2; ks++) {
      const bf16x8 a0 = lds_b128(sb + aoff + ks * 1024), a1 = lds_b128(sb + aoff + 2048 + ks * 1024);
      const char* bp = sb + boff + ks * 8192;
      const bf16x8 bg_ = lds_tr2(bp, bp + 256), bu_ = lds_tr2(bp + 2048, bp + 2304);
      ga[0] = mfma32(bg_, a0, ga[0]); ua[0] = mfma32(bu_, a0, ua[0]);
      ga[1] = mfma32(bg_, a1, ga[1]); ua[1] = mfma32(bu_, a1, ua[1]);
    }
    buf = buf == 2 ? 0 : buf + 1;
  }
#undef GU_STAGE
#pragma unroll
  for (int a = 0; a < 2; a++) {
    const int mblk = mb * 4 + mw * 2 + a;
#pragma unroll
    for (int hs = 0; hs < 2; hs++) {
      u32x4 o;
#pragma unroll
      for (int j = 0; j < 4; j++) {
        float r[2];
#pragma unroll
        for (int e = 0; e < 2; e++) {
          const float gv = ga[a][hs * 8 + 2 * j + e], uv = ua[a][hs * 8 + 2 * j + e];
          const float sg = __builtin_amdgcn_rcpf(1.0f + __builtin_amdgcn_exp2f(gv * -1.4426950408889634f));
          r[e] = gv * sg * uv;
        }
        o[j] = pk_bf16(r[0], r[1]);
      }
      const int ks = nb * 8 + nw * 2 + hs;
      st16(ws + WS_H + ((size_t)mblk * 112 + ks) * 1024, l * 16, o);
    }
  }
}

constexpr int K2_LDS = GU_LDS > AT_LDS + 256 ? GU_LDS : AT_LDS + 256;
__global__ __launch_bounds__(512, 4) void k2_mid(const u16* __restrict__ w1, const u16* __restrict__ w3, char* __restrict__ ws,
                                                 const u16* __restrict__ w2, const u16* __restrict__ wo) {
  __shared__ __attribute__((aligned(16))) char smem[K2_LDS];
  const int b = blockIdx.x, x = b & 7, j = b >> 3, a = j >> 1;
  if (((j + (j >> 5)) & 1) == 0) {
    __builtin_amdgcn_s_setprio(2);
    attn_body(smem, ws, ws, x * 4 + (a & 3), a >> 2, w2, wo);
  } else if (a < 28) {
    gateup_body(smem, w1, w3, ws, (x >> 1) * 4 + a / 7, (x & 1) * 7 + a % 7);
  }
}

constexpr int FN_D = 5;
constexpr int FN_RS = 68;
constexpr int FN_LDS = 8 * 64 * FN_RS * 4;

DEV void gld16(bf16x8& dst, const void* base, unsigned voff) {
  asm volatile("global_load_dwordx4 %0, %1, %2" : "=&v"(dst) : "v"(voff), "s"(base) : "memory");
}

__global__ __launch_bounds__(512, 2) void k3_final(const u16* __restrict__ x, const u16* __restrict__ w2, const u16* __restrict__ wo,
                                                   char* __restrict__ ws, u16* __restrict__ out) {
  __shared__ __attribute__((aligned(16))) char smem[FN_LDS];
  const int tid = threadIdx.x, l = tid & 63;
  const int w = __builtin_amdgcn_readfirstlane(tid >> 6);
  const int b = blockIdx.x, nb = (b >> 3) & 7, mb = (b & 7) * 4 + (b >> 6);
  const char* ah = ws + WS_H + ((size_t)(mb * 2) * 112 + w) * 1024;
  const char* ao = ws + WS_O + ((size_t)(mb * 2) * 32 + w) * 1024;
  const size_t brow = (size_t)(w * 16) * D + nb * 64;
  const unsigned bvo = ((l >> 3) * D + ((l & 7) ^ (((l >> 4) & 1) << 2)) * 8) * 2;
  const unsigned ring = lds_addr(smem) + w * (FN_D * 2048);
  bf16x8 fa[FN_D + 1][2];
#define FN_ISSUE(i)                                                            \
  do {                                                                         \
    const u16* p_ = ((i) < 14 ? w2 + (size_t)(i) * 128 * D : wo + (size_t)((i) - 14) * 128 * D) + brow; \
    glds16(p_, bvo, ring + ((i) % FN_D) * 2048);                               \
    glds16(p_ + 8 * D, bvo, ring + ((i) % FN_D) * 2048 + 1024);                \
    gld16(fa[(i) % (FN_D + 1)][0], (i) < 14 ? ah + (i) * 8192 : ao + ((i) - 14) * 8192, l * 16); \
    gld16(fa[(i) % (FN_D + 1)][1], (i) < 14 ? ah + (i) * 8192 + 112 * 1024 : ao + ((i) - 14) * 8192 + 32 * 1024, l * 16); \
  } while (0)
  const int tok = tid >> 3, n8 = (tid & 7) * 8;
  const size_t go = (size_t)(mb * 64 + tok) * D + nb * 64 + n8;
  const u32x4 xv = *(const u32x4*)(x + go);
#pragma unroll
  for (int i = 0; i < FN_D - 1; i++) FN_ISSUE(i);
  const int h = l >> 5, rr = (l & 15) >> 2;
  const char* bb[2];
#pragma unroll
  for (int nh = 0; nh < 2; nh++)
    bb[nh] = smem + w * (FN_D * 2048) + (4 * h + rr) * 128 + (((nh * 4 + ((l >> 4) & 1) * 2 + ((l & 3) >> 1)) ^ ((rr >> 1) << 2)) * 16) + (l & 1) * 8;
  f32x16 acc[2][2] = {};
  bf16x8 fb[2][2];
#pragma unroll
  for (int i = 0; i <= 18; i++) {
    if (i < 18) {
      bf16x8 &a0 = fa[i % (FN_D + 1)][0], &a1 = fa[i % (FN_D + 1)][1];
      const int fl = (17 - i < FN_D - 2 ? 17 - i : FN_D - 2) * 4;
      if (fl == 12) asm volatile("s_waitcnt vmcnt(12)" : "+v"(a0), "+v"(a1) :: "memory");
      else if (fl == 8) asm volatile("s_waitcnt vmcnt(8)" : "+v"(a0), "+v"(a1) :: "memory");
      else if (fl == 4) asm volatile("s_waitcnt vmcnt(4)" : "+v"(a0), "+v"(a1) :: "memory");
      else asm volatile("s_waitcnt vmcnt(0)" : "+v"(a0), "+v"(a1) :: "memory");
#pragma unroll
      for (int nh = 0; nh < 2; nh++) fb[i & 1][nh] = lds_tr2(bb[nh] + (i % FN_D) * 2048, bb[nh] + (i % FN_D) * 2048 + 1024);
    }
    if (i > 0) {
      const int z = (i - 1) % (FN_D + 1), y = (i - 1) & 1;
      acc[0][0] = mfma32(fb[y][0], fa[z][0], acc[0][0]);
      acc[1][0] = mfma32(fb[y][0], fa[z][1], acc[1][0]);
      acc[0][1] = mfma32(fb[y][1], fa[z][0], acc[0][1]);
      acc[1][1] = mfma32(fb[y][1], fa[z][1], acc[1][1]);
    }
    SB();
    if (i + FN_D - 1 < 18) FN_ISSUE(i + FN_D - 1);
    SB();
  }
#undef FN_ISSUE
  BAR();
#pragma unroll
  for (int a = 0; a < 2; a++)
#pragma unroll
    for (int nh = 0; nh < 2; nh++)
#pragma unroll
      for (int j = 0; j < 4; j++) {
        f32x4 v = {acc[a][nh][4 * j], acc[a][nh][4 * j + 1], acc[a][nh][4 * j + 2], acc[a][nh][4 * j + 3]};
        *(LDSP(f32x4))(smem + ((w * 64 + a * 32 + (l & 31)) * FN_RS + nh * 32 + 8 * j + 4 * h) * 4) = v;
      }
  BAR();
  f32x4 s0 = {}, s1 = {};
#pragma unroll
  for (int q = 0; q < 8; q++) {
    const char* p = smem + ((q * 64 + tok) * FN_RS + n8) * 4;
    s0 += *(const LDSP(f32x4))p;
    s1 += *(const LDSP(f32x4))(p + 16);
  }
  u32x4 o;
  o[0] = pk_bf16(s0[0] + bf_lo(xv[0]), s0[1] + bf_hi(xv[0]));
  o[1] = pk_bf16(s0[2] + bf_lo(xv[1]), s0[3] + bf_hi(xv[1]));
  o[2] = pk_bf16(s1[0] + bf_lo(xv[2]), s1[1] + bf_hi(xv[2]));
  o[3] = pk_bf16(s1[2] + bf_lo(xv[3]), s1[3] + bf_hi(xv[3]));
  st16((char*)out, (unsigned)go * 2, o);
}

extern "C" void kernel_launch(void* const* d_in, const int* in_sizes, int n_in, void* d_out, int out_size, void* d_ws,
                              size_t ws_size, hipStream_t stream) {
  const u16* x = (const u16*)d_in[0];
  const u16* wqkv = (const u16*)d_in[1];
  const u16* wo = (const u16*)d_in[2];
  const u16* w1 = (const u16*)d_in[4];
  const u16* w3 = (const u16*)d_in[5];
  const u16* w2 = (const u16*)d_in[6];
  char* ws = (char*)d_ws;
  k1_ln_qkv<<<256, 512, 0, stream>>>(x, wqkv, ws, w1, w3, (const char*)d_out);
  k2_mid<<<512, 512, 0, stream>>>(w1, w3, ws, w2, wo);
  k3_final<<<256, 512, 0, stream>>>(x, w2, wo, ws, (u16*)d_out);
}
```

```cpp
#include <hip/hip_runtime.h>
#include <stdint.h>

typedef __bf16 bf16x2 __attribute__((ext_vector_type(2)));
typedef __bf16 bf16x4 __attribute__((ext_vector_type(4)));
typedef __bf16 bf16x8 __attribute__((ext_vector_type(8)));
typedef __bf16 bf16x16 __attribute__((ext_vector_type(16)));
typedef float f32x2 __attribute__((ext_vector_type(2)));
typedef float f32x4 __attribute__((ext_vector_type(4)));
typedef float f32x8 __attribute__((ext_vector_type(8)));
typedef float f32x16 __attribute__((ext_vector_type(16)));
typedef short s16x4 __attribute__((ext_vector_type(4)));
typedef unsigned u32x2 __attribute__((ext_vector_type(2)));
typedef unsigned u32x4 __attribute__((ext_vector_type(4)));
typedef unsigned short u16;

#define DEV __device__ __forceinline__
#define LDSP(T) __attribute__((address_space(3))) T*
#define WAIT_V(n) asm volatile("s_waitcnt vmcnt(" #n ")" ::: "memory")
#define WAIT_L(n) asm volatile("s_waitcnt lgkmcnt(" #n ")" ::: "memory")
#define BAR() do { asm volatile("s_waitcnt lgkmcnt(0)" ::: "memory"); __builtin_amdgcn_s_barrier(); asm volatile("" ::: "memory"); } while (0)
#define BARK() do { asm volatile("" ::: "memory"); __builtin_amdgcn_s_waitcnt(0xC07F); __builtin_amdgcn_s_barrier(); asm volatile("" ::: "memory"); } while (0)
#define SB() __builtin_amdgcn_sched_barrier(0)

constexpr int S = 2048, D = 512, NH = 32, F = 1792;
constexpr size_t WS_XN = 0;
constexpr size_t WS_Q = WS_XN + 2u * S * D;
constexpr size_t WS_K = WS_Q + 2u * S * D;
constexpr size_t WS_V = WS_K + 2u * S * D;
constexpr size_t WS_O = WS_V + 2u * S * D;
constexpr size_t WS_H = WS_O + 2u * S * D;
constexpr size_t WS_B = WS_H + 2u * S * F;

DEV void glds16(const void* base, unsigned voff, unsigned lds) {
  unsigned keep;
  asm volatile("s_mov_b32 %0, m0\n\ts_mov_b32 m0, %3\n\ts_nop 0\n\tglobal_load_lds_dwordx4 %1, %2\n\ts_mov_b32 m0, %0"
               : "=&s"(keep) : "v"(voff), "s"(base), "s"(lds) : "memory");
}
DEV void touch(const void* base, unsigned voff, unsigned lds) {
  unsigned keep;
  asm volatile("s_mov_b32 %0, m0\n\ts_mov_b32 m0, %3\n\ts_nop 0\n\tglobal_load_lds_dword %1, %2\n\ts_mov_b32 m0, %0"
               : "=&s"(keep) : "v"(voff), "s"(base), "s"(lds) : "memory");
}
DEV void st16(char* base, unsigned off, u32x4 v) {
  const __amdgpu_buffer_rsrc_t rs = __builtin_amdgcn_make_buffer_rsrc(base, 0, 0x7fffffff, 0x00020000);
  __builtin_amdgcn_raw_buffer_store_b128(v, rs, (int)off, 0, 16);
}
DEV unsigned lds_addr(const char* p) { return (unsigned)(uintptr_t)(const LDSP(char))p; }
DEV bf16x8 lds_b128(const char* p) { return *(const LDSP(bf16x8))p; }
DEV bf16x8 lds_tr2(const char* p0, const char* p1) {
  s16x4 a = __builtin_amdgcn_ds_read_tr16_b64_v4i16((LDSP(s16x4))p0);
  s16x4 b = __builtin_amdgcn_ds_read_tr16_b64_v4i16((LDSP(s16x4))p1);
  return __builtin_bit_cast(bf16x8, __builtin_shufflevector(a, b, 0, 1, 2, 3, 4, 5, 6, 7));
}
DEV unsigned pk_bf16(float a, float b) {
  f32x2 v = {a, b};
  return __builtin_bit_cast(unsigned, __builtin_convertvector(v, bf16x2));
}
DEV float bf_lo(unsigned u) { return __builtin_bit_cast(float, u << 16); }
DEV float bf_hi(unsigned u) { return __builtin_bit_cast(float, u & 0xffff0000u); }
DEV float sum2_bf16(unsigned u, float acc) {
  return __builtin_amdgcn_fdot2_f32_bf16(__builtin_bit_cast(bf16x2, u), __builtin_bit_cast(bf16x2, 0x3F803F80u), acc, false);
}
template <int CTRL> DEV float dpp_add(float v) {
  return v + __builtin_bit_cast(float, __builtin_amdgcn_update_dpp(0, __builtin_bit_cast(int, v), CTRL, 0xf, 0xf, true));
}
DEV float row16_sum(float v) {
  v = dpp_add<0xB1>(v);
  v = dpp_add<0x4E>(v);
  v = dpp_add<0x141>(v);
  v = dpp_add<0x140>(v);
  return v;
}
DEV f32x16 mfma32(bf16x8 a, bf16x8 b, f32x16 c) { return __builtin_amdgcn_mfma_f32_32x32x16_bf16(a, b, c, 0, 0, 0); }
DEV f32x4 mfma16(bf16x8 a, bf16x8 b, f32x4 c) { return __builtin_amdgcn_mfma_f32_16x16x32_bf16(a, b, c, 0, 0, 0); }

constexpr int K1_A = 65536;
constexpr int K1_K8 = 3104;
constexpr int K1_ST = 8 * K1_K8;
constexpr int K1_LDS = K1_A + 3 * K1_ST + 256;

__global__ __launch_bounds__(512, 2) void k1_ln_qkv(const u16* __restrict__ x, const u16* __restrict__ wqkv, char* __restrict__ ws,
                                                    const u16* __restrict__ w1, const u16* __restrict__ w3) {
  __shared__ __attribute__((aligned(16))) char smem[K1_LDS];
  const int tid = threadIdx.x, l = tid & 63;
  const int w = __builtin_amdgcn_readfirstlane(tid >> 6);
  const int b = blockIdx.x, mb = ((b >> 1) & 3) * 8 + ((b >> 3) & 7), nb = (b & 1) * 4 + (b >> 6);
  const int r4 = l >> 4, c = l & 15;
  u32x4 xr[2][4];
#pragma unroll
  for (int g = 0; g < 2; g++)
#pragma unroll
    for (int i = 0; i < 4; i++)
      xr[g][i] = *(const u32x4*)(x + (size_t)(mb * 64 + w * 8 + g * 4 + r4) * D + i * 128 + c * 8);
  const u16* wsrc = wqkv + (size_t)(w * 8) * (3 * D) + nb * 64;
  const unsigned wvo = (((l & 31) >> 2) * (3 * D) + (l >> 5) * 32 + (l & 3) * 8) * 2;
  const unsigned bst = lds_addr(smem) + K1_A + w * K1_K8;
#define K1_STAGE(s, buf)                                                       \
  do {                                                                         \
    const u16* p_ = wsrc + (size_t)(s) * 64 * (3 * D);                         \
    glds16(p_, wvo, bst + (buf) * K1_ST);                                      \
    glds16(p_ + D, wvo, bst + (buf) * K1_ST + 1024);                           \
    glds16(p_ + 2 * D, wvo, bst + (buf) * K1_ST + 2048);                       \
  } while (0)
#define K1_PIECE(s, buf, i) glds16(wsrc + (size_t)(s) * 64 * (3 * D) + (i) * D, wvo, bst + (buf) * K1_ST + (i) * 1024)
  const unsigned dummy = lds_addr(smem) + K1_LDS - 256;
  float rsum[2];
#pragma unroll
  for (int g = 0; g < 2; g++) {
    float s0 = 0.f, s1 = 0.f;
#pragma unroll
    for (int i = 0; i < 4; i++)
#pragma unroll
      for (int j = 0; j < 4; j += 2) {
        s0 = sum2_bf16(xr[g][i][j], s0);
        s1 = sum2_bf16(xr[g][i][j + 1], s1);
      }
    rsum[g] = s0 + s1;
  }
  asm volatile("" : "+v"(rsum[0]), "+v"(rsum[1]));
  K1_STAGE(0, 0);
  K1_STAGE(1, 1);
  {
    const int i = min(w * 64 + l, 287), r = i / 6, q = i % 6;
    touch(wqkv, (unsigned)(((128 + (mb & 7) * 48 + r) * (3 * D) + (q >> 1) * D + nb * 64 + (q & 1) * 32) * 2), dummy);
  }
#pragma unroll
  for (int g = 0; g < 2; g++) {
    const float m = row16_sum(rsum[g]) * (1.0f / D);
    f32x2 v[16], q2 = {0.f, 0.f};
#pragma unroll
    for (int j = 0; j < 16; j++) {
      v[j] = f32x2{bf_lo(xr[g][j >> 2][j & 3]), bf_hi(xr[g][j >> 2][j & 3])} - f32x2{m, m};
      q2 += v[j] * v[j];
    }
    const float rs = __builtin_amdgcn_rsqf(row16_sum(q2[0] + q2[1]) * (1.0f / D) + 1e-5f);
    const int row = w * 8 + g * 4 + r4, tok = row & 31;
    u32x4 keep = {};
#pragma unroll
    for (int i = 0; i < 4; i++) {
      u32x4 o;
#pragma unroll
      for (int j = 0; j < 4; j++) { const f32x2 t = v[i * 4 + j] * f32x2{rs, rs}; o[j] = pk_bf16(t[0], t[1]); }
      const int ks = 8 * i + (c >> 1), half = c & 1;
      *(LDSP(u32x4))(smem + (row >> 5) * 32768 + (ks * 64 + half * 32 + (tok ^ (c & 7))) * 16) = o;
      if (i == (nb >> 1)) keep = o;
    }
    if ((c >> 3) == (nb & 1))
      st16(ws + WS_XN, (((mb * 2 + (row >> 5)) * 32 + 8 * (nb >> 1) + (c >> 1)) * 64 + (c & 1) * 32 + tok) * 16, keep);
  }
  {
    touch(w < 4 ? w1 : w3, (unsigned)((b * 112 + min((w & 3) * 32 + (l & 31), 111)) * 64), dummy);
  }
  const int mw = w & 1, nw = w >> 1, g = l >> 4;
  const char* ab[2];
#pragma unroll
  for (int p = 0; p < 2; p++)
    ab[p] = smem + mw * 32768 + (((g >> 1) * 64 + (g & 1) * 32) + ((l & 15) ^ (g | (4 * p)))) * 16;
  const char* bb = smem + K1_A + g * K1_K8 + (nw >> 1) * 512 + ((l & 15) >> 2) * 64 + (nw & 1) * 32 + (l & 3) * 8;
  f32x4 aq[2] = {}, ak[2] = {}, av[2] = {};
  bf16x8 fa[2][4], fb[2][6];
#pragma unroll
  for (int s = 0; s <= 8; s++) {
    const int z = (s - 1) & 1, y = s & 1;
    const bool rd = s < 8, mm = s > 0, dma = s + 2 < 8;
    if (rd) {
      if (s < 2) WAIT_V(7); else if (s < 7) WAIT_V(3); else WAIT_V(0);
      BARK();
    }
#define K1_RA(p, i) if (rd) fa[y][2 * (p) + (i)] = lds_b128(ab[p] + (2 * s + (p)) * 2048 + (i) * 256)
#define K1_RB(p, i) if (rd) { const char* bp = bb + (s % 3) * K1_ST + (p) * 4 * K1_K8 + (i) * 1024; fb[y][3 * (p) + (i)] = lds_tr2(bp, bp + 256); }
#define K1_MQ(p, i) if (mm) aq[i] = mfma16(fb[z][3 * (p)], fa[z][2 * (p) + (i)], aq[i])
#define K1_MK(p, i) if (mm) ak[i] = mfma16(fb[z][3 * (p) + 1], fa[z][2 * (p) + (i)], ak[i])
#define K1_MV(p, i) if (mm) av[i] = mfma16(fa[z][2 * (p) + (i)], fb[z][3 * (p) + 2], av[i])
    if (dma) K1_PIECE(s + 2, (s + 2) % 3, 0);
    K1_RA(0, 0); K1_RA(0, 1); K1_MQ(0, 0); SB();
    if (dma) K1_PIECE(s + 2, (s + 2) % 3, 1);
    K1_RB(0, 0); K1_MQ(0, 1); SB();
    if (dma) K1_PIECE(s + 2, (s + 2) % 3, 2);
    K1_RB(0, 1); K1_MK(0, 0); SB();
    K1_RB(0, 2); K1_MK(0, 1); SB();
    K1_RA(1, 0); K1_RA(1, 1); K1_MV(0, 0); SB();
    K1_RB(1, 0); K1_MV(0, 1); SB();
    K1_RB(1, 1); K1_MQ(1, 0); SB();
    K1_RB(1, 2); K1_MQ(1, 1); SB();
    K1_MK(1, 0); K1_MK(1, 1); K1_MV(1, 0); K1_MV(1, 1); SB();
  }
#undef K1_PIECE
  const int head = nb * 4 + nw, tile = mb * 2 + mw;
  const size_t tb = ((size_t)head * 64 + tile) * 1024;
  const float qs = 0.25f * 1.4426950408889634f;
  {
    unsigned q[4], k[4], v[4];
#pragma unroll
    for (int e = 0; e < 2; e++) {
      auto rq = __builtin_amdgcn_permlane16_swap(pk_bf16(aq[0][2 * e] * qs, aq[0][2 * e + 1] * qs), pk_bf16(aq[1][2 * e] * qs, aq[1][2 * e + 1] * qs), false, false);
      auto rk = __builtin_amdgcn_permlane16_swap(pk_bf16(ak[0][2 * e], ak[0][2 * e + 1]), pk_bf16(ak[1][2 * e], ak[1][2 * e + 1]), false, false);
      auto rv = __builtin_amdgcn_permlane16_swap(pk_bf16(av[0][2 * e], av[0][2 * e + 1]), pk_bf16(av[1][2 * e], av[1][2 * e + 1]), false, false);
      q[e] = rq[0]; q[2 + e] = rq[1]; k[e] = rk[0]; k[2 + e] = rk[1]; v[e] = rv[0]; v[2 + e] = rv[1];
    }
    st16(ws + WS_Q + tb, l * 16, u32x4{q[0], q[1], q[2], q[3]});
    st16(ws + WS_K + tb, l * 16, u32x4{k[0], k[1], k[2], k[3]});
    st16(ws + WS_V + tb, (g & 1) * 512 + (g >> 1) * 256 + (l & 15) * 16, u32x4{v[0], v[1], v[2], v[3]});
  }
#undef K1_STAGE
}

constexpr int AT_CH = 8 * 1024 + 8 * 1152;
constexpr int AT_LDS = 3 * AT_CH;

DEV f32x16 pv_step(bf16x8 v, f32x8 p, f32x16 acc) {
  return __builtin_amdgcn_smfmac_f32_32x32x32_bf16(v, __builtin_bit_cast(bf16x16, p), acc, (int)0xDDDDDDDD, 0, 0);
}

DEV void attn_body(char* smem, const char* ws_c, char* ws, int head, int qb) {
  const int tid = threadIdx.x, l = tid & 63;
  const int w = __builtin_amdgcn_readfirstlane(tid >> 6);
  const char* kg = ws_c + WS_K + (size_t)head * 65536;
  const char* vg = ws_c + WS_V + (size_t)head * 65536;
  const unsigned sm = lds_addr(smem);
  const int qt = qb * 8 + w;
  const bf16x8 qf = *(const bf16x8*)(ws_c + WS_Q + ((size_t)head * 64 + qt) * 1024 + l * 16);
  if (tid < 3 * 8 * 8) {
    const int t = tid >> 3;
    const unsigned o1 = (tid & 3) == 0 ? 0x3F803F80u : 0u;
    *(LDSP(u32x4))(smem + (t >> 3) * AT_CH + 8192 + (t & 7) * 1152 + 1024 + (tid & 7) * 16) = u32x4{o1, o1, o1, o1};
  }
#define AT_STAGE(ch, buf)                                                      \
  do {                                                                         \
    glds16(kg + ((ch) * 8 + w) * 1024, l * 16, sm + (buf) * AT_CH + w * 1024); \
    glds16(vg + ((ch) * 8 + w) * 1024, l * 16, sm + (buf) * AT_CH + 8192 + w * 1152);\
  } while (0)
  AT_STAGE(0, 0);
  AT_STAGE(1, 1);
  const int d = l & 31, ha = l >> 5;
  const int cst = 8192 + 1024 + ((d >= 20 && d < 28) ? 64 : 0) + ((d == 16 || d == 20) ? 0 : 16);
  const int va0 = d < 16 ? 8192 + (ha * 16 + d) * 16 : cst;
  const int va1 = d < 16 ? va0 + 512 : cst;
  f32x16 acc = {};
  WAIT_V(2);
  BAR();
  f32x16 sA = mfma32(lds_b128(smem + l * 16), qf, f32x16{});
  bf16x8 kn = lds_b128(smem + l * 16 + 1024);
  bf16x8 x0 = lds_b128(smem + va0), x1 = lds_b128(smem + va1);
  int buf = 0;
  for (int ch = 0; ch < 8; ch++) {
    const int nbuf = buf == 2 ? 0 : buf + 1;
    const char* cb = smem + buf * AT_CH;
    const char* cn = smem + (ch < 7 ? nbuf : buf) * AT_CH;
#pragma unroll
    for (int t = 0; t < 8; t++) {
      if (t == 6) {
        WAIT_V(0);
        BAR();
        if (ch + 2 < 8) { const int nb = buf >= 1 ? buf - 1 : 2; AT_STAGE(ch + 2, nb); }
      }
      f32x8 p0, p1;
#pragma unroll
      for (int j = 0; j < 4; j++) p0[j] = __builtin_amdgcn_exp2f(sA[j]);
      f32x16 sB = mfma32(kn, qf, f32x16{});
      kn = lds_b128((t + 2 < 8 ? cb + (t + 2) * 1024 : cn + (t + 2 - 8) * 1024) + l * 16);
#pragma unroll
      for (int j = 4; j < 8; j++) p0[j] = __builtin_amdgcn_exp2f(sA[j]);
      acc = pv_step(x0, p0, acc);
      x0 = lds_b128((t + 1 < 8 ? cb + (t + 1) * 1152 : cn) + va0);
#pragma unroll
      for (int j = 0; j < 8; j++) p1[j] = __builtin_amdgcn_exp2f(sA[8 + j]);
      acc = pv_step(x1, p1, acc);
      x1 = lds_b128((t + 1 < 8 ? cb + (t + 1) * 1152 : cn) + va1);
      sA = sB;
      __builtin_amdgcn_sched_group_barrier(0x400, 4, 0);
      __builtin_amdgcn_sched_group_barrier(0x008, 1, 0);
      __builtin_amdgcn_sched_group_barrier(0x100, 1, 0);
      __builtin_amdgcn_sched_group_barrier(0x400, 4, 0);
      __builtin_amdgcn_sched_group_barrier(0x008, 1, 0);
      __builtin_amdgcn_sched_group_barrier(0x100, 1, 0);
      __builtin_amdgcn_sched_group_barrier(0x400, 8, 0);
      __builtin_amdgcn_sched_group_barrier(0x008, 1, 0);
      __builtin_amdgcn_sched_group_barrier(0x100, 1, 0);
    }
    buf = nbuf;
  }
#undef AT_STAGE
  float lsum = acc[8];
  bool ok = lsum >= 0x1p-40f && lsum <= 0x1p40f;
#pragma unroll
  for (int j = 0; j < 8; j++) ok = ok && (__builtin_fabsf(acc[j]) < 0x1p120f);
  if (__builtin_amdgcn_ballot_w64(!ok) != 0) {
    float m = -__builtin_inff();
    acc = f32x16{};
    const bf16x8 ones = __builtin_bit_cast(bf16x8, u32x4{0x3F803F80u, 0x3F803F80u, 0x3F803F80u, 0x3F803F80u});
    const char* vl = ws_c + WS_V + (size_t)head * 65536 + (ha * 16 + (d & 15)) * 16;
    for (int t = 0; t < 64; t++) {
      const bf16x8 kf = *(const bf16x8*)(kg + t * 1024 + l * 16);
      bf16x8 x0 = *(const bf16x8*)(vl + t * 1024), x1 = *(const bf16x8*)(vl + t * 1024 + 512);
      if (d >= 16) { x0 = ones; x1 = ones; }
      f32x16 s = mfma32(kf, qf, f32x16{});
      float tm = s[0];
#pragma unroll
      for (int j = 1; j < 16; j++) tm = __builtin_fmaxf(tm, s[j]);
      tm = __builtin_fmaxf(tm, __shfl_xor(tm, 32));
      const float mn = __builtin_fmaxf(m, tm);
      const float al = __builtin_amdgcn_exp2f(m - mn);
      m = mn;
      f32x8 p0, p1;
#pragma unroll
      for (int j = 0; j < 8; j++) { p0[j] = __builtin_amdgcn_exp2f(s[j] - mn); p1[j] = __builtin_amdgcn_exp2f(s[8 + j] - mn); }
#pragma unroll
      for (int j = 0; j < 16; j++) acc[j] *= al;
      acc = pv_step(x0, p0, acc);
      acc = pv_step(x1, p1, acc);
    }
    lsum = acc[8];
  }
  const float inv = 1.0f / lsum;
  u32x4 o;
#pragma unroll
  for (int j = 0; j < 4; j++) o[j] = pk_bf16(acc[2 * j] * inv, acc[2 * j + 1] * inv);
  st16(ws + WS_O + ((size_t)qt * 32 + head) * 1024, l * 16, o);
}

constexpr int GU_ST = 8192 + 16384;
constexpr int GU_LDS = 3 * GU_ST;

DEV void gateup_body(char* smem, const u16* __restrict__ w1, const u16* __restrict__ w3, char* ws, int mb, int nb) {
  const int tid = threadIdx.x, l = tid & 63;
  const int w = __builtin_amdgcn_readfirstlane(tid >> 6);
  const char* ag = ws + WS_XN + ((size_t)(mb * 4 + (w >> 1)) * 32 + (w & 1)) * 1024;
  const unsigned sm = lds_addr(smem);
  const u16* bg = ((w & 1) ? w3 : w1) + (size_t)((w >> 1) * 8) * F + nb * 128;
  const unsigned bvo = (((l & 31) >> 2) * F + (l >> 5) * 32 + (l & 3) * 8) * 2;
#define GU_STAGE(s, buf)                                                       \
  do {                                                                         \
    glds16(ag + (s) * 2048, l * 16, sm + (buf) * GU_ST + w * 1024);            \
    const u16* p_ = bg + (size_t)(s) * 32 * F;                                 \
    glds16(p_, bvo, sm + (buf) * GU_ST + 8192 + w * 2048);                     \
    glds16(p_ + 64, bvo, sm + (buf) * GU_ST + 8192 + w * 2048 + 1024);         \
  } while (0)
  GU_STAGE(0, 0);
  GU_STAGE(1, 1);
  const int mw = w & 1, nw = w >> 1, h = l >> 5;
  const int aoff = mw * 4096 + l * 16;
  const int boff = 8192 + h * 4096 + nw * 512 + ((l & 15) >> 2) * 64 + ((l >> 4) & 1) * 32 + (l & 3) * 8;
  f32x16 ga[2] = {}, ua[2] = {};
  int buf = 0;
  for (int s = 0; s < 16; s++) {
    if (s < 15) WAIT_V(3); else WAIT_V(0);
    BAR();
    if (s + 2 < 16) { const int nbuf = buf >= 1 ? buf - 1 : 2; GU_STAGE(s + 2, nbuf); }
    const char* sb = smem + buf * GU_ST;
#pragma unroll
    for (int ks = 0; ks < 2; ks++) {
      const bf16x8 a0 = lds_b128(sb + aoff + ks * 1024), a1 = lds_b128(sb + aoff + 2048 + ks * 1024);
      const char* bp = sb + boff + ks * 8192;
      const bf16x8 bg_ = lds_tr2(bp, bp + 256), bu_ = lds_tr2(bp + 2048, bp + 2304);
      ga[0] = mfma32(bg_, a0, ga[0]); ua[0] = mfma32(bu_, a0, ua[0]);
      ga[1] = mfma32(bg_, a1, ga[1]); ua[1] = mfma32(bu_, a1, ua[1]);
    }
    buf = buf == 2 ? 0 : buf + 1;
  }
#undef GU_STAGE
#pragma unroll
  for (int a = 0; a < 2; a++) {
    const int mblk = mb * 4 + mw * 2 + a;
#pragma unroll
    for (int hs = 0; hs < 2; hs++) {
      u32x4 o;
#pragma unroll
      for (int j = 0; j < 4; j++) {
        float r[2];
#pragma unroll
        for (int e = 0; e < 2; e++) {
          const float gv = ga[a][hs * 8 + 2 * j + e], uv = ua[a][hs * 8 + 2 * j + e];
          const float sg = __builtin_amdgcn_rcpf(1.0f + __builtin_amdgcn_exp2f(gv * -1.4426950408889634f));
          r[e] = gv * sg * uv;
        }
        o[j] = pk_bf16(r[0], r[1]);
      }
      const int ks = nb * 8 + nw * 2 + hs;
      st16(ws + WS_H + ((size_t)mblk * 112 + ks) * 1024, l * 16, o);
    }
  }
}

DEV void repack_body(char* smem, const u16* __restrict__ w2, const u16* __restrict__ wo, char* ws, int id) {
  const int l = threadIdx.x & 63;
  const int w = __builtin_amdgcn_readfirstlane(threadIdx.x >> 6);
  const unsigned svo = ((l >> 3) * D + ((l & 7) ^ (((l >> 4) & 1) << 2)) * 8) * 2;
  const unsigned ring = lds_addr(smem) + w * 8192;
  const int h = l >> 5, rr = (l & 15) >> 2;
  for (int t0 = 0; t0 < 5; t0 += 4) {
    const int n = t0 == 0 ? 4 : (id < 16 ? 1 : 0);
    for (int t = 0; t < n; t++) {
      const int s = id + 32 * (t0 + t);
      const u16* p = (s < 112 ? w2 + (size_t)s * 16 * D : wo + (size_t)(s - 112) * 16 * D) + w * 64;
      glds16(p, svo, ring + t * 2048);
      glds16(p + 8 * D, svo, ring + t * 2048 + 1024);
    }
    WAIT_V(0);
    for (int t = 0; t < n; t++) {
      const int s = id + 32 * (t0 + t);
#pragma unroll
      for (int nh = 0; nh < 2; nh++) {
        const char* bp = smem + w * 8192 + t * 2048 + (4 * h + rr) * 128 +
                         (((nh * 4 + ((l >> 4) & 1) * 2 + ((l & 3) >> 1)) ^ ((rr >> 1) << 2)) * 16) + (l & 1) * 8;
        st16(ws + WS_B, ((w * 144 + s) * 2 + nh) * 1024 + l * 16, __builtin_bit_cast(u32x4, lds_tr2(bp, bp + 1024)));
      }
    }
  }
}

constexpr int K2_LDS = GU_LDS > AT_LDS ? GU_LDS : AT_LDS;
static_assert(K2_LDS >= 8 * 8192, "repack ring");
__global__ __launch_bounds__(512, 4) void k2_mid(const u16* __restrict__ w1, const u16* __restrict__ w3, char* __restrict__ ws,
                                                 const u16* __restrict__ w2, const u16* __restrict__ wo) {
  __shared__ __attribute__((aligned(16))) char smem[K2_LDS];
  const int b = blockIdx.x, x = b & 7, j = b >> 3, a = j >> 1;
  if (((j + (j >> 5)) & 1) == 0) {
    __builtin_amdgcn_s_setprio(2);
    attn_body(smem, ws, ws, x * 4 + (a & 3), a >> 2);
  } else if (a < 28) {
    gateup_body(smem, w1, w3, ws, (x >> 1) * 4 + a / 7, (x & 1) * 7 + a % 7);
  } else {
    repack_body(smem, w2, wo, ws, x * 4 + (a - 28));
  }
}

constexpr int FN_D = 5;

DEV void gld16(bf16x8& dst, const void* base, unsigned voff) {
  asm volatile("global_load_dwordx4 %0, %1, %2" : "=&v"(dst) : "v"(voff), "s"(base) : "memory");
}
template <int N> DEV void wait_v4(bf16x8 (&f)[4]) {
  asm volatile("s_waitcnt vmcnt(%4)" : "+v"(f[0]), "+v"(f[1]), "+v"(f[2]), "+v"(f[3]) : "n"(N) : "memory");
}

__global__ __launch_bounds__(512, 2) void k3_final(const u16* __restrict__ x, char* __restrict__ ws, u16* __restrict__ out) {
  __shared__ __attribute__((aligned(16))) char smem[8 * 7 * 2048];
  const int l = threadIdx.x & 63;
  const int w = __builtin_amdgcn_readfirstlane(threadIdx.x >> 6);
  const int wa = w & 1, wn = (w >> 1) & 1, we = w >> 2;
  const int b = blockIdx.x, nb = (b >> 3) & 7, mb = (b & 7) * 4 + (b >> 6);
  const char* ah0 = ws + WS_H + ((size_t)(mb * 2 + wa) * 112 + w) * 1024;
  const char* ah1 = ws + WS_H + ((size_t)(mb * 2 + (wa ^ 1)) * 112 + w) * 1024;
  const char* ao0 = ws + WS_O + ((size_t)(mb * 2 + wa) * 32 + w) * 1024;
  const char* ao1 = ws + WS_O + ((size_t)(mb * 2 + (wa ^ 1)) * 32 + w) * 1024;
  const char* bf0 = ws + WS_B + ((size_t)(nb * 144 + w) * 2 + wn) * 1024;
  const char* bf1 = ws + WS_B + ((size_t)(nb * 144 + w) * 2 + (wn ^ 1)) * 1024;
  const unsigned va = l * 16, vb = (l ^ (we * 16)) * 16;
  bf16x8 f[FN_D][4];
#define FN_ISSUE(i, q)                                                         \
  gld16(f[(i) % FN_D][q], (q) == 0 ? ((i) < 14 ? ah0 + (i) * 8192 : ao0 + ((i) - 14) * 8192)   \
                        : (q) == 1 ? ((i) < 14 ? ah1 + (i) * 8192 : ao1 + ((i) - 14) * 8192)   \
                        : (q) == 2 ? bf0 + (i) * 16384 : bf1 + (i) * 16384, (q) < 2 ? va : vb)
  const int h = l >> 5;
  const unsigned go = ((mb * 64 + wa * 32 + (l & 31)) * D + nb * 64 + wn * 32 + we * 16 + 8 * h) * 2;
  bf16x8 xv;
  gld16(xv, x, go);
#pragma unroll
  for (int i = 0; i < FN_D - 1; i++)
#pragma unroll
    for (int q = 0; q < 4; q++) FN_ISSUE(i, q);
  f32x16 acc[4] = {};
#pragma unroll
  for (int i = 0; i < 18; i++) {
    bf16x8(&c)[4] = f[i % FN_D];
    switch (17 - i < FN_D - 2 ? 17 - i : FN_D - 2) {
      case 3: wait_v4<12>(c); break;
      case 2: wait_v4<8>(c); break;
      case 1: wait_v4<4>(c); break;
      default: wait_v4<0>(c); break;
    }
    acc[0] = mfma32(c[2], c[0], acc[0]);
    acc[1] = mfma32(c[2], c[1], acc[1]);
    acc[2] = mfma32(c[3], c[0], acc[2]);
    acc[3] = mfma32(c[3], c[1], acc[3]);
    SB();
    if (i + FN_D - 1 < 18) {
#pragma unroll
      for (int q = 0; q < 4; q++) FN_ISSUE(i + FN_D - 1, q);
    }
    SB();
  }
#undef FN_ISSUE
  static_assert(FN_D == 5, "wait counts");
#pragma unroll
  for (int c = 1; c < 8; c++)
#pragma unroll
    for (int j = 0; j < 2; j++) {
      const int o = (c >> 2) * 8 + j * 4;
      *(LDSP(f32x4))(smem + (((w ^ c) * 7 + c - 1) * 2 + j) * 1024 + l * 16) =
          f32x4{acc[c & 3][o], acc[c & 3][o + 1], acc[c & 3][o + 2], acc[c & 3][o + 3]};
    }
  BAR();
  asm volatile("" : "+v"(xv));
  f32x4 s[2];
#pragma unroll
  for (int j = 0; j < 2; j++) {
    s[j] = f32x4{acc[0][4 * j], acc[0][4 * j + 1], acc[0][4 * j + 2], acc[0][4 * j + 3]};
#pragma unroll
    for (int c = 1; c < 8; c++) s[j] += *(const LDSP(f32x4))(smem + ((w * 7 + c - 1) * 2 + j) * 1024 + l * 16);
  }
  float v[8];
#pragma unroll
  for (int q = 0; q < 4; q++) {
    auto t = __builtin_amdgcn_permlane32_swap(__builtin_bit_cast(unsigned, s[0][q] + 0.f), __builtin_bit_cast(unsigned, s[1][q] + 0.f), false, false);
    v[q] = __builtin_bit_cast(float, (unsigned)t[0]);
    v[4 + q] = __builtin_bit_cast(float, (unsigned)t[1]);
  }
  const u32x4 xr = __builtin_bit_cast(u32x4, xv);
  u32x4 o;
#pragma unroll
  for (int q = 0; q < 4; q++) o[q] = pk_bf16(v[2 * q] + bf_lo(xr[q]), v[2 * q + 1] + bf_hi(xr[q]));
  st16((char*)out, go, o);
}

extern "C" void kernel_launch(void* const* d_in, const int* in_sizes, int n_in, void* d_out, int out_size, void* d_ws,
                              size_t ws_size, hipStream_t stream) {
  const u16* x = (const u16*)d_in[0];
  const u16* wqkv = (const u16*)d_in[1];
  const u16* wo = (const u16*)d_in[2];
  const u16* w1 = (const u16*)d_in[4];
  const u16* w3 = (const u16*)d_in[5];
  const u16* w2 = (const u16*)d_in[6];
  char* ws = (char*)d_ws;
  k1_ln_qkv<<<256, 512, 0, stream>>>(x, wqkv, ws, w1, w3);
  k2_mid<<<512, 512, 0, stream>>>(w1, w3, ws, w2, wo);
  k3_final<<<256, 512, 0, stream>>>(x, ws, (u16*)d_out);
}
```

```cpp
#include <hip/hip_runtime.h>
#include <stdint.h>

typedef __bf16 bf16x2 __attribute__((ext_vector_type(2)));
typedef __bf16 bf16x4 __attribute__((ext_vector_type(4)));
typedef __bf16 bf16x8 __attribute__((ext_vector_type(8)));
typedef __bf16 bf16x16 __attribute__((ext_vector_type(16)));
typedef float f32x2 __attribute__((ext_vector_type(2)));
typedef float f32x4 __attribute__((ext_vector_type(4)));
typedef float f32x8 __attribute__((ext_vector_type(8)));
typedef float f32x16 __attribute__((ext_vector_type(16)));
typedef short s16x4 __attribute__((ext_vector_type(4)));
typedef unsigned u32x2 __attribute__((ext_vector_type(2)));
typedef unsigned u32x4 __attribute__((ext_vector_type(4)));
typedef unsigned short u16;

#define DEV __device__ __forceinline__
#define LDSP(T) __attribute__((address_space(3))) T*
#define WAIT_V(n) asm volatile("s_waitcnt vmcnt(" #n ")" ::: "memory")
#define WAIT_L(n) asm volatile("s_waitcnt lgkmcnt(" #n ")" ::: "memory")
#define BAR() do { asm volatile("s_waitcnt lgkmcnt(0)" ::: "memory"); __builtin_amdgcn_s_barrier(); asm volatile("" ::: "memory"); } while (0)
#define BARK() do { asm volatile("" ::: "memory"); __builtin_amdgcn_s_waitcnt(0xC07F); __builtin_amdgcn_s_barrier(); asm volatile("" ::: "memory"); } while (0)
#define SB() __builtin_amdgcn_sched_barrier(0)

constexpr int S = 2048, D = 512, NH = 32, F = 1792;
constexpr size_t WS_XN = 0;
constexpr size_t WS_Q = WS_XN + 2u * S * D;
constexpr size_t WS_K = WS_Q + 2u * S * D;
constexpr size_t WS_V = WS_K + 2u * S * D;
constexpr size_t WS_O = WS_V + 2u * S * D;
constexpr size_t WS_H = WS_O + 2u * S * D;
constexpr size_t WS_B = WS_H + 2u * S * F;

DEV void glds16(const void* base, unsigned voff, unsigned lds) {
  unsigned keep;
  asm volatile("s_mov_b32 %0, m0\n\ts_mov_b32 m0, %3\n\ts_nop 0\n\tglobal_load_lds_dwordx4 %1, %2\n\ts_mov_b32 m0, %0"
               : "=&s"(keep) : "v"(voff), "s"(base), "s"(lds) : "memory");
}
DEV void touch(const void* base, unsigned voff, unsigned lds) {
  unsigned keep;
  asm volatile("s_mov_b32 %0, m0\n\ts_mov_b32 m0, %3\n\ts_nop 0\n\tglobal_load_lds_dword %1, %2\n\ts_mov_b32 m0, %0"
               : "=&s"(keep) : "v"(voff), "s"(base), "s"(lds) : "memory");
}
DEV void st16(char* base, unsigned off, u32x4 v) {
  const __amdgpu_buffer_rsrc_t rs = __builtin_amdgcn_make_buffer_rsrc(base, 0, 0x7fffffff, 0x00020000);
  __builtin_amdgcn_raw_buffer_store_b128(v, rs, (int)off, 0, 16);
}
DEV unsigned lds_addr(const char* p) { return (unsigned)(uintptr_t)(const LDSP(char))p; }
DEV bf16x8 lds_b128(const char* p) { return *(const LDSP(bf16x8))p; }
DEV bf16x8 lds_tr2(const char* p0, const char* p1) {
  s16x4 a = __builtin_amdgcn_ds_read_tr16_b64_v4i16((LDSP(s16x4))p0);
  s16x4 b = __builtin_amdgcn_ds_read_tr16_b64_v4i16((LDSP(s16x4))p1);
  return __builtin_bit_cast(bf16x8, __builtin_shufflevector(a, b, 0, 1, 2, 3, 4, 5, 6, 7));
}
DEV unsigned pk_bf16(float a, float b) {
  f32x2 v = {a, b};
  return __builtin_bit_cast(unsigned, __builtin_convertvector(v, bf16x2));
}
DEV float bf_lo(unsigned u) { return __builtin_bit_cast(float, u << 16); }
DEV float bf_hi(unsigned u) { return __builtin_bit_cast(float, u & 0xffff0000u); }
DEV float sum2_bf16(unsigned u, float acc) {
  return __builtin_amdgcn_fdot2_f32_bf16(__builtin_bit_cast(bf16x2, u), __builtin_bit_cast(bf16x2, 0x3F803F80u), acc, false);
}
template <int CTRL> DEV float dpp_add(float v) {
  return v + __builtin_bit_cast(float, __builtin_amdgcn_update_dpp(0, __builtin_bit_cast(int, v), CTRL, 0xf, 0xf, true));
}
DEV float row16_sum(float v) {
  v = dpp_add<0xB1>(v);
  v = dpp_add<0x4E>(v);
  v = dpp_add<0x141>(v);
  v = dpp_add<0x140>(v);
  return v;
}
DEV f32x16 mfma32(bf16x8 a, bf16x8 b, f32x16 c) { return __builtin_amdgcn_mfma_f32_32x32x16_bf16(a, b, c, 0, 0, 0); }
DEV f32x4 mfma16(bf16x8 a, bf16x8 b, f32x4 c) { return __builtin_amdgcn_mfma_f32_16x16x32_bf16(a, b, c, 0, 0, 0); }

constexpr int K1_A = 65536;
constexpr int K1_K8 = 3104;
constexpr int K1_ST = 8 * K1_K8;
constexpr int K1_LDS = K1_A + 3 * K1_ST + 256;

__global__ __launch_bounds__(512, 2) void k1_ln_qkv(const u16* __restrict__ x, const u16* __restrict__ wqkv, char* __restrict__ ws,
                                                    const u16* __restrict__ w1, const u16* __restrict__ w3) {
  __shared__ __attribute__((aligned(16))) char smem[K1_LDS];
  const int tid = threadIdx.x, l = tid & 63;
  const int w = __builtin_amdgcn_readfirstlane(tid >> 6);
  const int b = blockIdx.x, mb = ((b >> 1) & 3) * 8 + ((b >> 3) & 7), nb = (b & 1) * 4 + (b >> 6);
  const int r4 = l >> 4, c = l & 15;
  u32x4 xr[2][4];
#pragma unroll
  for (int g = 0; g < 2; g++)
#pragma unroll
    for (int i = 0; i < 4; i++)
      xr[g][i] = *(const u32x4*)(x + (size_t)(mb * 64 + w * 8 + g * 4 + r4) * D + i * 128 + c * 8);
  const u16* wsrc = wqkv + (size_t)(w * 8) * (3 * D) + nb * 64;
  const unsigned wvo = (((l & 31) >> 2) * (3 * D) + (l >> 5) * 32 + (l & 3) * 8) * 2;
  const unsigned bst = lds_addr(smem) + K1_A + w * K1_K8;
#define K1_STAGE(s, buf)                                                       \
  do {                                                                         \
    const u16* p_ = wsrc + (size_t)(s) * 64 * (3 * D);                         \
    glds16(p_, wvo, bst + (buf) * K1_ST);                                      \
    glds16(p_ + D, wvo, bst + (buf) * K1_ST + 1024);                           \
    glds16(p_ + 2 * D, wvo, bst + (buf) * K1_ST + 2048);                       \
  } while (0)
#define K1_PIECE(s, buf, i) glds16(wsrc + (size_t)(s) * 64 * (3 * D) + (i) * D, wvo, bst + (buf) * K1_ST + (i) * 1024)
  const unsigned dummy = lds_addr(smem) + K1_LDS - 256;
  float rsum[2];
#pragma unroll
  for (int g = 0; g < 2; g++) {
    float s0 = 0.f, s1 = 0.f;
#pragma unroll
    for (int i = 0; i < 4; i++)
#pragma unroll
      for (int j = 0; j < 4; j += 2) {
        s0 = sum2_bf16(xr[g][i][j], s0);
        s1 = sum2_bf16(xr[g][i][j + 1], s1);
      }
    rsum[g] = s0 + s1;
  }
  asm volatile("" : "+v"(rsum[0]), "+v"(rsum[1]));
  K1_STAGE(0, 0);
  K1_STAGE(1, 1);
  {
    const int i = min(w * 64 + l, 287), r = i / 6, q = i % 6;
    touch(wqkv, (unsigned)(((128 + (mb & 7) * 48 + r) * (3 * D) + (q >> 1) * D + nb * 64 + (q & 1) * 32) * 2), dummy);
  }
#pragma unroll
  for (int g = 0; g < 2; g++) {
    const float m = row16_sum(rsum[g]) * (1.0f / D);
    f32x2 v[16], q2 = {0.f, 0.f};
#pragma unroll
    for (int j = 0; j < 16; j++) {
      v[j] = f32x2{bf_lo(xr[g][j >> 2][j & 3]), bf_hi(xr[g][j >> 2][j & 3])} - f32x2{m, m};
      q2 += v[j] * v[j];
    }
    const float var = row16_sum(q2[0] + q2[1]) * (1.0f / D);
    const float ve = bf_lo(pk_bf16(var, var)) + 1e-5f;
    const float sd = __builtin_amdgcn_sqrtf(bf_lo(pk_bf16(ve, ve)));
    const float rs = __builtin_amdgcn_rcpf(bf_lo(pk_bf16(sd, sd)));
    const int row = w * 8 + g * 4 + r4, tok = row & 31;
    u32x4 keep = {};
#pragma unroll
    for (int i = 0; i < 4; i++) {
      u32x4 o;
#pragma unroll
      for (int j = 0; j < 4; j++) { const f32x2 t = v[i * 4 + j] * f32x2{rs, rs}; o[j] = pk_bf16(t[0], t[1]); }
      const int ks = 8 * i + (c >> 1), half = c & 1;
      *(LDSP(u32x4))(smem + (row >> 5) * 32768 + (ks * 64 + half * 32 + (tok ^ (c & 7))) * 16) = o;
      if (i == (nb >> 1)) keep = o;
    }
    if ((c >> 3) == (nb & 1))
      st16(ws + WS_XN, (((mb * 2 + (row >> 5)) * 32 + 8 * (nb >> 1) + (c >> 1)) * 64 + (c & 1) * 32 + tok) * 16, keep);
  }
  {
    touch(w < 4 ? w1 : w3, (unsigned)((b * 112 + min((w & 3) * 32 + (l & 31), 111)) * 64), dummy);
  }
  const int mw = w & 1, nw = w >> 1, g = l >> 4;
  const char* ab[2];
#pragma unroll
  for (int p = 0; p < 2; p++)
    ab[p] = smem + mw * 32768 + (((g >> 1) * 64 + (g & 1) * 32) + ((l & 15) ^ (g | (4 * p)))) * 16;
  const char* bb = smem + K1_A + g * K1_K8 + (nw >> 1) * 512 + ((l & 15) >> 2) * 64 + (nw & 1) * 32 + (l & 3) * 8;
  f32x4 aq[2] = {}, ak[2] = {}, av[2] = {};
  bf16x8 fa[2][4], fb[2][6];
#pragma unroll
  for (int s = 0; s <= 8; s++) {
    const int z = (s - 1) & 1, y = s & 1;
    const bool rd = s < 8, mm = s > 0, dma = s + 2 < 8;
    if (rd) {
      if (s < 2) WAIT_V(7); else if (s < 7) WAIT_V(3); else WAIT_V(0);
      BARK();
    }
#define K1_RA(p, i) if (rd) fa[y][2 * (p) + (i)] = lds_b128(ab[p] + (2 * s + (p)) * 2048 + (i) * 256)
#define K1_RB(p, i) if (rd) { const char* bp = bb + (s % 3) * K1_ST + (p) * 4 * K1_K8 + (i) * 1024; fb[y][3 * (p) + (i)] = lds_tr2(bp, bp + 256); }
#define K1_MQ(p, i) if (mm) aq[i] = mfma16(fb[z][3 * (p)], fa[z][2 * (p) + (i)], aq[i])
#define K1_MK(p, i) if (mm) ak[i] = mfma16(fb[z][3 * (p) + 1], fa[z][2 * (p) + (i)], ak[i])
#define K1_MV(p, i) if (mm) av[i] = mfma16(fa[z][2 * (p) + (i)], fb[z][3 * (p) + 2], av[i])
    if (dma) K1_PIECE(s + 2, (s + 2) % 3, 0);
    K1_RA(0, 0); K1_RA(0, 1); K1_MQ(0, 0); SB();
    if (dma) K1_PIECE(s + 2, (s + 2) % 3, 1);
    K1_RB(0, 0); K1_MQ(0, 1); SB();
    if (dma) K1_PIECE(s + 2, (s + 2) % 3, 2);
    K1_RB(0, 1); K1_MK(0, 0); SB();
    K1_RB(0, 2); K1_MK(0, 1); SB();
    K1_RA(1, 0); K1_RA(1, 1); K1_MV(0, 0); SB();
    K1_RB(1, 0); K1_MV(0, 1); SB();
    K1_RB(1, 1); K1_MQ(1, 0); SB();
    K1_RB(1, 2); K1_MQ(1, 1); SB();
    K1_MK(1, 0); K1_MK(1, 1); K1_MV(1, 0); K1_MV(1, 1); SB();
  }
#undef K1_PIECE
  const int head = nb * 4 + nw, tile = mb * 2 + mw;
  const size_t tb = ((size_t)head * 64 + tile) * 1024;
  const float qs = 0.25f * 1.4426950408889634f;
  {
    unsigned q[4], k[4], v[4];
#pragma unroll
    for (int e = 0; e < 2; e++) {
      auto rq = __builtin_amdgcn_permlane16_swap(pk_bf16(aq[0][2 * e] * qs, aq[0][2 * e + 1] * qs), pk_bf16(aq[1][2 * e] * qs, aq[1][2 * e + 1] * qs), false, false);
      auto rk = __builtin_amdgcn_permlane16_swap(pk_bf16(ak[0][2 * e], ak[0][2 * e + 1]), pk_bf16(ak[1][2 * e], ak[1][2 * e + 1]), false, false);
      auto rv = __builtin_amdgcn_permlane16_swap(pk_bf16(av[0][2 * e], av[0][2 * e + 1]), pk_bf16(av[1][2 * e], av[1][2 * e + 1]), false, false);
      q[e] = rq[0]; q[2 + e] = rq[1]; k[e] = rk[0]; k[2 + e] = rk[1]; v[e] = rv[0]; v[2 + e] = rv[1];
    }
    st16(ws + WS_Q + tb, l * 16, u32x4{q[0], q[1], q[2], q[3]});
    st16(ws + WS_K + tb, l * 16, u32x4{k[0], k[1], k[2], k[3]});
    st16(ws + WS_V + tb, (g & 1) * 512 + (g >> 1) * 256 + (l & 15) * 16, u32x4{v[0], v[1], v[2], v[3]});
  }
#undef K1_STAGE
}

constexpr int AT_CH = 8 * 1024 + 8 * 1152;
constexpr int AT_LDS = 3 * AT_CH;

DEV f32x16 pv_step(bf16x8 v, f32x8 p, f32x16 acc) {
  return __builtin_amdgcn_smfmac_f32_32x32x32_bf16(v, __builtin_bit_cast(bf16x16, p), acc, (int)0xDDDDDDDD, 0, 0);
}

DEV void attn_body(char* smem, const char* ws_c, char* ws, int head, int qb) {
  const int tid = threadIdx.x, l = tid & 63;
  const int w = __builtin_amdgcn_readfirstlane(tid >> 6);
  const char* kg = ws_c + WS_K + (size_t)head * 65536;
  const char* vg = ws_c + WS_V + (size_t)head * 65536;
  const unsigned sm = lds_addr(smem);
  const int qt = qb * 8 + w;
  const bf16x8 qf = *(const bf16x8*)(ws_c + WS_Q + ((size_t)head * 64 + qt) * 1024 + l * 16);
  if (tid < 3 * 8 * 8) {
    const int t = tid >> 3;
    const unsigned o1 = (tid & 3) == 0 ? 0x3F803F80u : 0u;
    *(LDSP(u32x4))(smem + (t >> 3) * AT_CH + 8192 + (t & 7) * 1152 + 1024 + (tid & 7) * 16) = u32x4{o1, o1, o1, o1};
  }
#define AT_STAGE(ch, buf)                                                      \
  do {                                                                         \
    glds16(kg + ((ch) * 8 + w) * 1024, l * 16, sm + (buf) * AT_CH + w * 1024); \
    glds16(vg + ((ch) * 8 + w) * 1024, l * 16, sm + (buf) * AT_CH + 8192 + w * 1152);\
  } while (0)
  AT_STAGE(0, 0);
  AT_STAGE(1, 1);
  const int d = l & 31, ha = l >> 5;
  const int cst = 8192 + 1024 + ((d >= 20 && d < 28) ? 64 : 0) + ((d == 16 || d == 20) ? 0 : 16);
  const int va0 = d < 16 ? 8192 + (ha * 16 + d) * 16 : cst;
  const int va1 = d < 16 ? va0 + 512 : cst;
  f32x16 acc = {};
  WAIT_V(2);
  BAR();
  f32x16 sA = mfma32(lds_b128(smem + l * 16), qf, f32x16{});
  bf16x8 kn = lds_b128(smem + l * 16 + 1024);
  bf16x8 x0 = lds_b128(smem + va0), x1 = lds_b128(smem + va1);
  int buf = 0;
  for (int ch = 0; ch < 8; ch++) {
    const int nbuf = buf == 2 ? 0 : buf + 1;
    const char* cb = smem + buf * AT_CH;
    const char* cn = smem + (ch < 7 ? nbuf : buf) * AT_CH;
#pragma unroll
    for (int t = 0; t < 8; t++) {
      if (t == 6) {
        WAIT_V(0);
        BAR();
        if (ch + 2 < 8) { const int nb = buf >= 1 ? buf - 1 : 2; AT_STAGE(ch + 2, nb); }
      }
      f32x8 p0, p1;
#pragma unroll
      for (int j = 0; j < 4; j++) p0[j] = __builtin_amdgcn_exp2f(sA[j]);
      f32x16 sB = mfma32(kn, qf, f32x16{});
      kn = lds_b128((t + 2 < 8 ? cb + (t + 2) * 1024 : cn + (t + 2 - 8) * 1024) + l * 16);
#pragma unroll
      for (int j = 4; j < 8; j++) p0[j] = __builtin_amdgcn_exp2f(sA[j]);
      acc = pv_step(x0, p0, acc);
      x0 = lds_b128((t + 1 < 8 ? cb + (t + 1) * 1152 : cn) + va0);
#pragma unroll
      for (int j = 0; j < 8; j++) p1[j] = __builtin_amdgcn_exp2f(sA[8 + j]);
      acc = pv_step(x1, p1, acc);
      x1 = lds_b128((t + 1 < 8 ? cb + (t + 1) * 1152 : cn) + va1);
      sA = sB;
      __builtin_amdgcn_sched_group_barrier(0x400, 4, 0);
      __builtin_amdgcn_sched_group_barrier(0x008, 1, 0);
      __builtin_amdgcn_sched_group_barrier(0x100, 1, 0);
      __builtin_amdgcn_sched_group_barrier(0x400, 4, 0);
      __builtin_amdgcn_sched_group_barrier(0x008, 1, 0);
      __builtin_amdgcn_sched_group_barrier(0x100, 1, 0);
      __builtin_amdgcn_sched_group_barrier(0x400, 8, 0);
      __builtin_amdgcn_sched_group_barrier(0x008, 1, 0);
      __builtin_amdgcn_sched_group_barrier(0x100, 1, 0);
    }
    buf = nbuf;
  }
#undef AT_STAGE
  float lsum = acc[8];
  bool ok = lsum >= 0x1p-40f && lsum <= 0x1p40f;
#pragma unroll
  for (int j = 0; j < 8; j++) ok = ok && (__builtin_fabsf(acc[j]) < 0x1p120f);
  if (__builtin_amdgcn_ballot_w64(!ok) != 0) {
    float m = -__builtin_inff();
    acc = f32x16{};
    const bf16x8 ones = __builtin_bit_cast(bf16x8, u32x4{0x3F803F80u, 0x3F803F80u, 0x3F803F80u, 0x3F803F80u});
    const char* vl = ws_c + WS_V + (size_t)head * 65536 + (ha * 16 + (d & 15)) * 16;
    for (int t = 0; t < 64; t++) {
      const bf16x8 kf = *(const bf16x8*)(kg + t * 1024 + l * 16);
      bf16x8 x0 = *(const bf16x8*)(vl + t * 1024), x1 = *(const bf16x8*)(vl + t * 1024 + 512);
      if (d >= 16) { x0 = ones; x1 = ones; }
      f32x16 s = mfma32(kf, qf, f32x16{});
      float tm = s[0];
#pragma unroll
      for (int j = 1; j < 16; j++) tm = __builtin_fmaxf(tm, s[j]);
      tm = __builtin_fmaxf(tm, __shfl_xor(tm, 32));
      const float mn = __builtin_fmaxf(m, tm);
      const float al = __builtin_amdgcn_exp2f(m - mn);
      m = mn;
      f32x8 p0, p1;
#pragma unroll
      for (int j = 0; j < 8; j++) { p0[j] = __builtin_amdgcn_exp2f(s[j] - mn); p1[j] = __builtin_amdgcn_exp2f(s[8 + j] - mn); }
#pragma unroll
      for (int j = 0; j < 16; j++) acc[j] *= al;
      acc = pv_step(x0, p0, acc);
      acc = pv_step(x1, p1, acc);
    }
    lsum = acc[8];
  }
  const float inv = 1.0f / lsum;
  u32x4 o;
#pragma unroll
  for (int j = 0; j < 4; j++) o[j] = pk_bf16(acc[2 * j] * inv, acc[2 * j + 1] * inv);
  st16(ws + WS_O + ((size_t)qt * 32 + head) * 1024, l * 16, o);
}

constexpr int GU_ST = 8192 + 16384;
constexpr int GU_LDS = 3 * GU_ST;

DEV void gateup_body(char* smem, const u16* __restrict__ w1, const u16* __restrict__ w3, char* ws, int mb, int nb) {
  const int tid = threadIdx.x, l = tid & 63;
  const int w = __builtin_amdgcn_readfirstlane(tid >> 6);
  const char* ag = ws + WS_XN + ((size_t)(mb * 4 + (w >> 1)) * 32 + (w & 1)) * 1024;
  const unsigned sm = lds_addr(smem);
  const u16* bg = ((w & 1) ? w3 : w1) + (size_t)((w >> 1) * 8) * F + nb * 128;
  const unsigned bvo = (((l & 31) >> 2) * F + (l >> 5) * 32 + (l & 3) * 8) * 2;
#define GU_STAGE(s, buf)                                                       \
  do {                                                                         \
    glds16(ag + (s) * 2048, l * 16, sm + (buf) * GU_ST + w * 1024);            \
    const u16* p_ = bg + (size_t)(s) * 32 * F;                                 \
    glds16(p_, bvo, sm + (buf) * GU_ST + 8192 + w * 2048);                     \
    glds16(p_ + 64, bvo, sm + (buf) * GU_ST + 8192 + w * 2048 + 1024);         \
  } while (0)
  GU_STAGE(0, 0);
  GU_STAGE(1, 1);
  const int mw = w & 1, nw = w >> 1, h = l >> 5;
  const int aoff = mw * 4096 + l * 16;
  const int boff = 8192 + h * 4096 + nw * 512 + ((l & 15) >> 2) * 64 + ((l >> 4) & 1) * 32 + (l & 3) * 8;
  f32x16 ga[2] = {}, ua[2] = {};
  int buf = 0;
  for (int s = 0; s < 16; s++) {
    if (s < 15) WAIT_V(3); else WAIT_V(0);
    BAR();
    if (s + 2 < 16) { const int nbuf = buf >= 1 ? buf - 1 : 2; GU_STAGE(s + 2, nbuf); }
    const char* sb = smem + buf * GU_ST;
#pragma unroll
    for (int ks = 0; ks < 2; ks++) {
      const bf16x8 a0 = lds_b128(sb + aoff + ks * 1024), a1 = lds_b128(sb + aoff + 2048 + ks * 1024);
      const char* bp = sb + boff + ks * 8192;
      const bf16x8 bg_ = lds_tr2(bp, bp + 256), bu_ = lds_tr2(bp + 2048, bp + 2304);
      ga[0] = mfma32(bg_, a0, ga[0]); ua[0] = mfma32(bu_, a0, ua[0]);
      ga[1] = mfma32(bg_, a1, ga[1]); ua[1] = mfma32(bu_, a1, ua[1]);
    }
    buf = buf == 2 ? 0 : buf + 1;
  }
#undef GU_STAGE
#pragma unroll
  for (int a = 0; a < 2; a++) {
    const int mblk = mb * 4 + mw * 2 + a;
#pragma unroll
    for (int hs = 0; hs < 2; hs++) {
      u32x4 o;
#pragma unroll
      for (int j = 0; j < 4; j++) {
        float r[2];
#pragma unroll
        for (int e = 0; e < 2; e++) {
          const float gv = ga[a][hs * 8 + 2 * j + e], uv = ua[a][hs * 8 + 2 * j + e];
          const float sg = __builtin_amdgcn_rcpf(1.0f + __builtin_amdgcn_exp2f(gv * -1.4426950408889634f));
          r[e] = gv * sg * uv;
        }
        o[j] = pk_bf16(r[0], r[1]);
      }
      const int ks = nb * 8 + nw * 2 + hs;
      st16(ws + WS_H + ((size_t)mblk * 112 + ks) * 1024, l * 16, o);
    }
  }
}

DEV void repack_body(char* smem, const u16* __restrict__ w2, const u16* __restrict__ wo, char* ws, int id) {
  const int l = threadIdx.x & 63;
  const int w = __builtin_amdgcn_readfirstlane(threadIdx.x >> 6);
  const unsigned svo = ((l >> 3) * D + ((l & 7) ^ (((l >> 4) & 1) << 2)) * 8) * 2;
  const unsigned ring = lds_addr(smem) + w * 8192;
  const int h = l >> 5, rr = (l & 15) >> 2;
  for (int t0 = 0; t0 < 5; t0 += 4) {
    const int n = t0 == 0 ? 4 : (id < 16 ? 1 : 0);
    for (int t = 0; t < n; t++) {
      const int s = id + 32 * (t0 + t);
      const u16* p = (s < 112 ? w2 + (size_t)s * 16 * D : wo + (size_t)(s - 112) * 16 * D) + w * 64;
      glds16(p, svo, ring + t * 2048);
      glds16(p + 8 * D, svo, ring + t * 2048 + 1024);
    }
    WAIT_V(0);
    for (int t = 0; t < n; t++) {
      const int s = id + 32 * (t0 + t);
#pragma unroll
      for (int nh = 0; nh < 2; nh++) {
        const char* bp = smem + w * 8192 + t * 2048 + (4 * h + rr) * 128 +
                         (((nh * 4 + ((l >> 4) & 1) * 2 + ((l & 3) >> 1)) ^ ((rr >> 1) << 2)) * 16) + (l & 1) * 8;
        st16(ws + WS_B, ((w * 144 + s) * 2 + nh) * 1024 + l * 16, __builtin_bit_cast(u32x4, lds_tr2(bp, bp + 1024)));
      }
    }
  }
}

constexpr int K2_LDS = GU_LDS > AT_LDS ? GU_LDS : AT_LDS;
static_assert(K2_LDS >= 8 * 8192, "repack ring");
__global__ __launch_bounds__(512, 4) void k2_mid(const u16* __restrict__ w1, const u16* __restrict__ w3, char* __restrict__ ws,
                                                 const u16* __restrict__ w2, const u16* __restrict__ wo) {
  __shared__ __attribute__((aligned(16))) char smem[K2_LDS];
  const int b = blockIdx.x, x = b & 7, j = b >> 3, a = j & 31;
  if (j < 32) {
    __builtin_amdgcn_s_setprio(2);
    attn_body(smem, ws, ws, x * 4 + (a & 3), a >> 2);
  } else if (a < 28) {
    gateup_body(smem, w1, w3, ws, (x >> 1) * 4 + a / 7, (x & 1) * 7 + a % 7);
  } else {
    repack_body(smem, w2, wo, ws, x * 4 + (a - 28));
  }
}

constexpr int FN_D = 5;

DEV void gld16(bf16x8& dst, const void* base, unsigned voff) {
  asm volatile("global_load_dwordx4 %0, %1, %2" : "=&v"(dst) : "v"(voff), "s"(base) : "memory");
}
template <int N> DEV void wait_v4(bf16x8 (&f)[4]) {
  asm volatile("s_waitcnt vmcnt(%4)" : "+v"(f[0]), "+v"(f[1]), "+v"(f[2]), "+v"(f[3]) : "n"(N) : "memory");
}

__global__ __launch_bounds__(512, 2) void k3_final(const u16* __restrict__ x, char* __restrict__ ws, u16* __restrict__ out) {
  __shared__ __attribute__((aligned(16))) char smem[8 * 7 * 2048];
  const int l = threadIdx.x & 63;
  const int w = __builtin_amdgcn_readfirstlane(threadIdx.x >> 6);
  const int wa = w & 1, wn = (w >> 1) & 1, we = w >> 2;
  const int b = blockIdx.x, nb = (b >> 3) & 7, mb = (b & 7) * 4 + (b >> 6);
  const char* ah0 = ws + WS_H + ((size_t)(mb * 2 + wa) * 112 + w) * 1024;
  const char* ah1 = ws + WS_H + ((size_t)(mb * 2 + (wa ^ 1)) * 112 + w) * 1024;
  const char* ao0 = ws + WS_O + ((size_t)(mb * 2 + wa) * 32 + w) * 1024;
  const char* ao1 = ws + WS_O + ((size_t)(mb * 2 + (wa ^ 1)) * 32 + w) * 1024;
  const char* bf0 = ws + WS_B + ((size_t)(nb * 144 + w) * 2 + wn) * 1024;
  const char* bf1 = ws + WS_B + ((size_t)(nb * 144 + w) * 2 + (wn ^ 1)) * 1024;
  const unsigned va = l * 16, vb = (l ^ (we * 16)) * 16;
  bf16x8 f[FN_D][4];
#define FN_ISSUE(i, q)                                                         \
  gld16(f[(i) % FN_D][q], (q) == 0 ? ((i) < 14 ? ah0 + (i) * 8192 : ao0 + ((i) - 14) * 8192)   \
                        : (q) == 1 ? ((i) < 14 ? ah1 + (i) * 8192 : ao1 + ((i) - 14) * 8192)   \
                        : (q) == 2 ? bf0 + (i) * 16384 : bf1 + (i) * 16384, (q) < 2 ? va : vb)
  const int h = l >> 5;
  const unsigned go = ((mb * 64 + wa * 32 + (l & 31)) * D + nb * 64 + wn * 32 + we * 16 + 8 * h) * 2;
  bf16x8 xv;
  gld16(xv, x, go);
#pragma unroll
  for (int i = 0; i < FN_D - 1; i++)
#pragma unroll
    for (int q = 0; q < 4; q++) FN_ISSUE(i, q);
  f32x16 acc[4] = {};
#pragma unroll
  for (int i = 0; i < 18; i++) {
    bf16x8(&c)[4] = f[i % FN_D];
    switch (17 - i < FN_D - 2 ? 17 - i : FN_D - 2) {
      case 3: wait_v4<12>(c); break;
      case 2: wait_v4<8>(c); break;
      case 1: wait_v4<4>(c); break;
      default: wait_v4<0>(c); break;
    }
    acc[0] = mfma32(c[2], c[0], acc[0]);
    acc[1] = mfma32(c[2], c[1], acc[1]);
    acc[2] = mfma32(c[3], c[0], acc[2]);
    acc[3] = mfma32(c[3], c[1], acc[3]);
    SB();
    if (i + FN_D - 1 < 18) {
#pragma unroll
      for (int q = 0; q < 4; q++) FN_ISSUE(i + FN_D - 1, q);
    }
    SB();
  }
#undef FN_ISSUE
  static_assert(FN_D == 5, "wait counts");
#pragma unroll
  for (int c = 1; c < 8; c++)
#pragma unroll
    for (int j = 0; j < 2; j++) {
      const int o = (c >> 2) * 8 + j * 4;
      *(LDSP(f32x4))(smem + (((w ^ c) * 7 + c - 1) * 2 + j) * 1024 + l * 16) =
          f32x4{acc[c & 3][o], acc[c & 3][o + 1], acc[c & 3][o + 2], acc[c & 3][o + 3]};
    }
  BAR();
  asm volatile("" : "+v"(xv));
  f32x4 s[2];
#pragma unroll
  for (int j = 0; j < 2; j++) {
    s[j] = f32x4{acc[0][4 * j], acc[0][4 * j + 1], acc[0][4 * j + 2], acc[0][4 * j + 3]};
#pragma unroll
    for (int c = 1; c < 8; c++) s[j] += *(const LDSP(f32x4))(smem + ((w * 7 + c - 1) * 2 + j) * 1024 + l * 16);
  }
  float v[8];
#pragma unroll
  for (int q = 0; q < 4; q++) {
    auto t = __builtin_amdgcn_permlane32_swap(__builtin_bit_cast(unsigned, s[0][q] + 0.f), __builtin_bit_cast(unsigned, s[1][q] + 0.f), false, false);
    v[q] = __builtin_bit_cast(float, (unsigned)t[0]);
    v[4 + q] = __builtin_bit_cast(float, (unsigned)t[1]);
  }
  const u32x4 xr = __builtin_bit_cast(u32x4, xv);
  u32x4 o;
#pragma unroll
  for (int q = 0; q < 4; q++) o[q] = pk_bf16(v[2 * q] + bf_lo(xr[q]), v[2 * q + 1] + bf_hi(xr[q]));
  st16((char*)out, go, o);
}

extern "C" void kernel_launch(void* const* d_in, const int* in_sizes, int n_in, void* d_out, int out_size, void* d_ws,
                              size_t ws_size, hipStream_t stream) {
  const u16* x = (const u16*)d_in[0];
  const u16* wqkv = (const u16*)d_in[1];
  const u16* wo = (const u16*)d_in[2];
  const u16* w1 = (const u16*)d_in[4];
  const u16* w3 = (const u16*)d_in[5];
  const u16* w2 = (const u16*)d_in[6];
  char* ws = (char*)d_ws;
  k1_ln_qkv<<<256, 512, 0, stream>>>(x, wqkv, ws, w1, w3);
  k2_mid<<<512, 512, 0, stream>>>(w1, w3, ws, w2, wo);
  k3_final<<<256, 512, 0, stream>>>(x, ws, (u16*)d_out);
}
```

```cpp
#include <hip/hip_runtime.h>
#include <stdint.h>

typedef __bf16 bf16x2 __attribute__((ext_vector_type(2)));
typedef __bf16 bf16x4 __attribute__((ext_vector_type(4)));
typedef __bf16 bf16x8 __attribute__((ext_vector_type(8)));
typedef __bf16 bf16x16 __attribute__((ext_vector_type(16)));
typedef float f32x2 __attribute__((ext_vector_type(2)));
typedef float f32x4 __attribute__((ext_vector_type(4)));
typedef float f32x8 __attribute__((ext_vector_type(8)));
typedef float f32x16 __attribute__((ext_vector_type(16)));
typedef short s16x4 __attribute__((ext_vector_type(4)));
typedef unsigned u32x2 __attribute__((ext_vector_type(2)));
typedef unsigned u32x4 __attribute__((ext_vector_type(4)));
typedef unsigned short u16;

#define DEV __device__ __forceinline__
#define LDSP(T) __attribute__((address_space(3))) T*
#define WAIT_V(n) asm volatile("s_waitcnt vmcnt(" #n ")" ::: "memory")
#define WAIT_L(n) asm volatile("s_waitcnt lgkmcnt(" #n ")" ::: "memory")
#define BAR() do { asm volatile("s_waitcnt lgkmcnt(0)" ::: "memory"); __builtin_amdgcn_s_barrier(); asm volatile("" ::: "memory"); } while (0)
#define BARK() do { asm volatile("" ::: "memory"); __builtin_amdgcn_s_waitcnt(0xC07F); __builtin_amdgcn_s_barrier(); asm volatile("" ::: "memory"); } while (0)
#define SB() __builtin_amdgcn_sched_barrier(0)

constexpr int S = 2048, D = 512, NH = 32, F = 1792;
constexpr size_t WS_XN = 0;
constexpr size_t WS_Q = WS_XN + 2u * S * D;
constexpr size_t WS_K = WS_Q + 2u * S * D;
constexpr size_t WS_V = WS_K + 2u * S * D;
constexpr size_t WS_O = WS_V + 2u * S * D;
constexpr size_t WS_H = WS_O + 2u * S * D;
constexpr size_t WS_B = WS_H + 2u * S * F;

DEV void glds16(const void* base, unsigned voff, unsigned lds) {
  unsigned keep;
  asm volatile("s_mov_b32 %0, m0\n\ts_mov_b32 m0, %3\n\ts_nop 0\n\tglobal_load_lds_dwordx4 %1, %2\n\ts_mov_b32 m0, %0"
               : "=&s"(keep) : "v"(voff), "s"(base), "s"(lds) : "memory");
}
DEV void touch(const void* base, unsigned voff, unsigned lds) {
  unsigned keep;
  asm volatile("s_mov_b32 %0, m0\n\ts_mov_b32 m0, %3\n\ts_nop 0\n\tglobal_load_lds_dword %1, %2\n\ts_mov_b32 m0, %0"
               : "=&s"(keep) : "v"(voff), "s"(base), "s"(lds) : "memory");
}
DEV void st16(char* base, unsigned off, u32x4 v) {
  const __amdgpu_buffer_rsrc_t rs = __builtin_amdgcn_make_buffer_rsrc(base, 0, 0x7fffffff, 0x00020000);
  __builtin_amdgcn_raw_buffer_store_b128(v, rs, (int)off, 0, 16);
}
DEV unsigned lds_addr(const char* p) { return (unsigned)(uintptr_t)(const LDSP(char))p; }
DEV bf16x8 lds_b128(const char* p) { return *(const LDSP(bf16x8))p; }
DEV bf16x8 lds_tr2(const char* p0, const char* p1) {
  s16x4 a = __builtin_amdgcn_ds_read_tr16_b64_v4i16((LDSP(s16x4))p0);
  s16x4 b = __builtin_amdgcn_ds_read_tr16_b64_v4i16((LDSP(s16x4))p1);
  return __builtin_bit_cast(bf16x8, __builtin_shufflevector(a, b, 0, 1, 2, 3, 4, 5, 6, 7));
}
DEV unsigned pk_bf16(float a, float b) {
  f32x2 v = {a, b};
  return __builtin_bit_cast(unsigned, __builtin_convertvector(v, bf16x2));
}
DEV float bf_lo(unsigned u) { return __builtin_bit_cast(float, u << 16); }
DEV float bf_hi(unsigned u) { return __builtin_bit_cast(float, u & 0xffff0000u); }
DEV float sum2_bf16(unsigned u, float acc) {
  return __builtin_amdgcn_fdot2_f32_bf16(__builtin_bit_cast(bf16x2, u), __builtin_bit_cast(bf16x2, 0x3F803F80u), acc, false);
}
template <int CTRL> DEV float dpp_add(float v) {
  return v + __builtin_bit_cast(float, __builtin_amdgcn_update_dpp(0, __builtin_bit_cast(int, v), CTRL, 0xf, 0xf, true));
}
DEV float row16_sum(float v) {
  v = dpp_add<0xB1>(v);
  v = dpp_add<0x4E>(v);
  v = dpp_add<0x141>(v);
  v = dpp_add<0x140>(v);
  return v;
}
DEV f32x16 mfma32(bf16x8 a, bf16x8 b, f32x16 c) { return __builtin_amdgcn_mfma_f32_32x32x16_bf16(a, b, c, 0, 0, 0); }
DEV f32x4 mfma16(bf16x8 a, bf16x8 b, f32x4 c) { return __builtin_amdgcn_mfma_f32_16x16x32_bf16(a, b, c, 0, 0, 0); }

constexpr int K1_A = 65536;
constexpr int K1_K8 = 3104;
constexpr int K1_ST = 8 * K1_K8;
constexpr int K1_LDS = K1_A + 3 * K1_ST + 256;

__global__ __launch_bounds__(512, 2) void k1_ln_qkv(const u16* __restrict__ x, const u16* __restrict__ wqkv, char* __restrict__ ws,
                                                    const u16* __restrict__ w1, const u16* __restrict__ w3) {
  __shared__ __attribute__((aligned(16))) char smem[K1_LDS];
  const int tid = threadIdx.x, l = tid & 63;
  const int w = __builtin_amdgcn_readfirstlane(tid >> 6);
  const int b = blockIdx.x, mb = ((b >> 1) & 3) * 8 + ((b >> 3) & 7), nb = (b & 1) * 4 + (b >> 6);
  const int r4 = l >> 4, c = l & 15;
  u32x4 xr[2][4];
#pragma unroll
  for (int g = 0; g < 2; g++)
#pragma unroll
    for (int i = 0; i < 4; i++)
      xr[g][i] = *(const u32x4*)(x + (size_t)(mb * 64 + w * 8 + g * 4 + r4) * D + i * 128 + c * 8);
  const u16* wsrc = wqkv + (size_t)(w * 8) * (3 * D) + nb * 64;
  const unsigned wvo = (((l & 31) >> 2) * (3 * D) + (l >> 5) * 32 + (l & 3) * 8) * 2;
  const unsigned bst = lds_addr(smem) + K1_A + w * K1_K8;
#define K1_STAGE(s, buf)                                                       \
  do {                                                                         \
    const u16* p_ = wsrc + (size_t)(s) * 64 * (3 * D);                         \
    glds16(p_, wvo, bst + (buf) * K1_ST);                                      \
    glds16(p_ + D, wvo, bst + (buf) * K1_ST + 1024);                           \
    glds16(p_ + 2 * D, wvo, bst + (buf) * K1_ST + 2048);                       \
  } while (0)
#define K1_PIECE(s, buf, i) glds16(wsrc + (size_t)(s) * 64 * (3 * D) + (i) * D, wvo, bst + (buf) * K1_ST + (i) * 1024)
  const unsigned dummy = lds_addr(smem) + K1_LDS - 256;
  float rsum[2];
#pragma unroll
  for (int g = 0; g < 2; g++) {
    float s0 = 0.f, s1 = 0.f;
#pragma unroll
    for (int i = 0; i < 4; i++)
#pragma unroll
      for (int j = 0; j < 4; j += 2) {
        s0 = sum2_bf16(xr[g][i][j], s0);
        s1 = sum2_bf16(xr[g][i][j + 1], s1);
      }
    rsum[g] = s0 + s1;
  }
  asm volatile("" : "+v"(rsum[0]), "+v"(rsum[1]));
  K1_STAGE(0, 0);
  K1_STAGE(1, 1);
  {
    const int i = min(w * 64 + l, 287), r = i / 6, q = i % 6;
    touch(wqkv, (unsigned)(((128 + (mb & 7) * 48 + r) * (3 * D) + (q >> 1) * D + nb * 64 + (q & 1) * 32) * 2), dummy);
  }
#pragma unroll
  for (int g = 0; g < 2; g++) {
    const float m = row16_sum(rsum[g]) * (1.0f / D);
    f32x2 v[16], q2 = {0.f, 0.f};
#pragma unroll
    for (int j = 0; j < 16; j++) {
      v[j] = f32x2{bf_lo(xr[g][j >> 2][j & 3]), bf_hi(xr[g][j >> 2][j & 3])} - f32x2{m, m};
      q2 += v[j] * v[j];
    }
    const float var = row16_sum(q2[0] + q2[1]) * (1.0f / D);
    const float ve = bf_lo(pk_bf16(var, var)) + 1e-5f;
    const float sd = __builtin_amdgcn_sqrtf(bf_lo(pk_bf16(ve, ve)));
    const float rs = __builtin_amdgcn_rcpf(bf_lo(pk_bf16(sd, sd)));
    const int row = w * 8 + g * 4 + r4, tok = row & 31;
    u32x4 keep = {};
#pragma unroll
    for (int i = 0; i < 4; i++) {
      u32x4 o;
#pragma unroll
      for (int j = 0; j < 4; j++) { const f32x2 t = v[i * 4 + j] * f32x2{rs, rs}; o[j] = pk_bf16(t[0], t[1]); }
      const int ks = 8 * i + (c >> 1), half = c & 1;
      *(LDSP(u32x4))(smem + (row >> 5) * 32768 + (ks * 64 + half * 32 + (tok ^ (c & 7))) * 16) = o;
      if (i == (nb >> 1)) keep = o;
    }
    if ((c >> 3) == (nb & 1))
      st16(ws + WS_XN, (((mb * 2 + (row >> 5)) * 32 + 8 * (nb >> 1) + (c >> 1)) * 64 + (c & 1) * 32 + tok) * 16, keep);
  }
  {
    touch(w < 4 ? w1 : w3, (unsigned)((b * 112 + min((w & 3) * 32 + (l & 31), 111)) * 64), dummy);
  }
  const int mw = w & 1, nw = w >> 1, g = l >> 4;
  const char* ab[2];
#pragma unroll
  for (int p = 0; p < 2; p++)
    ab[p] = smem + mw * 32768 + (((g >> 1) * 64 + (g & 1) * 32) + ((l & 15) ^ (g | (4 * p)))) * 16;
  const char* bb = smem + K1_A + g * K1_K8 + (nw >> 1) * 512 + ((l & 15) >> 2) * 64 + (nw & 1) * 32 + (l & 3) * 8;
  f32x4 aq[2] = {}, ak[2] = {}, av[2] = {};
  bf16x8 fa[2][4], fb[2][6];
#pragma unroll
  for (int s = 0; s <= 8; s++) {
    const int z = (s - 1) & 1, y = s & 1;
    const bool rd = s < 8, mm = s > 0, dma = s + 2 < 8;
    if (rd) {
      if (s < 2) WAIT_V(7); else if (s < 7) WAIT_V(3); else WAIT_V(0);
      BARK();
    }
#define K1_RA(p, i) if (rd) fa[y][2 * (p) + (i)] = lds_b128(ab[p] + (2 * s + (p)) * 2048 + (i) * 256)
#define K1_RB(p, i) if (rd) { const char* bp = bb + (s % 3) * K1_ST + (p) * 4 * K1_K8 + (i) * 1024; fb[y][3 * (p) + (i)] = lds_tr2(bp, bp + 256); }
#define K1_MQ(p, i) if (mm) aq[i] = mfma16(fb[z][3 * (p)], fa[z][2 * (p) + (i)], aq[i])
#define K1_MK(p, i) if (mm) ak[i] = mfma16(fb[z][3 * (p) + 1], fa[z][2 * (p) + (i)], ak[i])
#define K1_MV(p, i) if (mm) av[i] = mfma16(fa[z][2 * (p) + (i)], fb[z][3 * (p) + 2], av[i])
    if (dma) K1_PIECE(s + 2, (s + 2) % 3, 0);
    K1_RA(0, 0); K1_RA(0, 1); K1_MQ(0, 0); SB();
    if (dma) K1_PIECE(s + 2, (s + 2) % 3, 1);
    K1_RB(0, 0); K1_MQ(0, 1); SB();
    if (dma) K1_PIECE(s + 2, (s + 2) % 3, 2);
    K1_RB(0, 1); K1_MK(0, 0); SB();
    K1_RB(0, 2); K1_MK(0, 1); SB();
    K1_RA(1, 0); K1_RA(1, 1); K1_MV(0, 0); SB();
    K1_RB(1, 0); K1_MV(0, 1); SB();
    K1_RB(1, 1); K1_MQ(1, 0); SB();
    K1_RB(1, 2); K1_MQ(1, 1); SB();
    K1_MK(1, 0); K1_MK(1, 1); K1_MV(1, 0); K1_MV(1, 1); SB();
  }
#undef K1_PIECE
  const int head = nb * 4 + nw, tile = mb * 2 + mw;
  const size_t tb = ((size_t)head * 64 + tile) * 1024;
  const float qs = 0.25f * 1.4426950408889634f;
  {
    unsigned q[4], k[4], v[4];
#pragma unroll
    for (int e = 0; e < 2; e++) {
      auto rq = __builtin_amdgcn_permlane16_swap(pk_bf16(aq[0][2 * e] * qs, aq[0][2 * e + 1] * qs), pk_bf16(aq[1][2 * e] * qs, aq[1][2 * e + 1] * qs), false, false);
      auto rk = __builtin_amdgcn_permlane16_swap(pk_bf16(ak[0][2 * e], ak[0][2 * e + 1]), pk_bf16(ak[1][2 * e], ak[1][2 * e + 1]), false, false);
      auto rv = __builtin_amdgcn_permlane16_swap(pk_bf16(av[0][2 * e], av[0][2 * e + 1]), pk_bf16(av[1][2 * e], av[1][2 * e + 1]), false, false);
      q[e] = rq[0]; q[2 + e] = rq[1]; k[e] = rk[0]; k[2 + e] = rk[1]; v[e] = rv[0]; v[2 + e] = rv[1];
    }
    st16(ws + WS_Q + tb, l * 16, u32x4{q[0], q[1], q[2], q[3]});
    st16(ws + WS_K + tb, l * 16, u32x4{k[0], k[1], k[2], k[3]});
    st16(ws + WS_V + tb, (g & 1) * 512 + (g >> 1) * 256 + (l & 15) * 16, u32x4{v[0], v[1], v[2], v[3]});
  }
#undef K1_STAGE
}

constexpr int AT_CH = 8 * 1024 + 8 * 1152;
constexpr int AT_LDS = 3 * AT_CH;

DEV f32x16 pv_step(bf16x8 v, f32x8 p, f32x16 acc) {
  return __builtin_amdgcn_smfmac_f32_32x32x32_bf16(v, __builtin_bit_cast(bf16x16, p), acc, (int)0xDDDDDDDD, 0, 0);
}

DEV void attn_body(char* smem, const char* ws_c, char* ws, int head, int qb) {
  const int tid = threadIdx.x, l = tid & 63;
  const int w = __builtin_amdgcn_readfirstlane(tid >> 6);
  const char* kg = ws_c + WS_K + (size_t)head * 65536;
  const char* vg = ws_c + WS_V + (size_t)head * 65536;
  const unsigned sm = lds_addr(smem);
  const int qg = w & 3, kh = w >> 2;
  const int qt = qb * 8 + 2 * qg + kh;
  const char* qp = ws_c + WS_Q + (size_t)head * 65536 + l * 16;
  const bf16x8 qa = *(const bf16x8*)(qp + (qt ^ 1) * 1024);
  const bf16x8 qf = *(const bf16x8*)(qp + qt * 1024);
  if (tid < 3 * 8 * 8) {
    const int t = tid >> 3;
    const unsigned o1 = (tid & 3) == 0 ? 0x3F803F80u : 0u;
    *(LDSP(u32x4))(smem + (t >> 3) * AT_CH + 8192 + (t & 7) * 1152 + 1024 + (tid & 7) * 16) = u32x4{o1, o1, o1, o1};
  }
#define AT_STAGE(ch, buf)                                                      \
  do {                                                                         \
    glds16(kg + ((ch) * 8 + w) * 1024, l * 16, sm + (buf) * AT_CH + w * 1024); \
    glds16(vg + ((ch) * 8 + w) * 1024, l * 16, sm + (buf) * AT_CH + 8192 + w * 1152);\
  } while (0)
  AT_STAGE(0, 0);
  AT_STAGE(1, 1);
  const int d = l & 31, ha = l >> 5;
  const int cst = 8192 + 1024 + ((d >= 20 && d < 28) ? 64 : 0) + ((d == 16 || d == 20) ? 0 : 16);
  const int va0 = (d < 16 ? 8192 + (ha * 16 + d) * 16 : cst) + kh * 4 * 1152;
  const int va1 = d < 16 ? va0 + 512 : va0;
  const int ka = l * 16 + kh * 4096;
  f32x16 accs[2] = {};
  WAIT_V(2);
  BAR();
  bf16x8 kn = lds_b128(smem + ka);
  bf16x8 x0 = lds_b128(smem + va0), x1 = lds_b128(smem + va1);
  f32x16 sA = mfma32(kn, qa, f32x16{});
  int buf = 0;
  for (int ch = 0; ch < 8; ch++) {
    const int nbuf = buf == 2 ? 0 : buf + 1;
    const char* cb = smem + buf * AT_CH;
    const char* cn = smem + (ch < 7 ? nbuf : buf) * AT_CH;
#pragma unroll
    for (int t = 0; t < 8; t++) {
      if (t == 6) {
        WAIT_V(0);
        BAR();
        if (ch + 2 < 8) { const int nb = buf >= 1 ? buf - 1 : 2; AT_STAGE(ch + 2, nb); }
      }
      const int u = (t >> 1) + 1;
      f32x8 p0, p1;
#pragma unroll
      for (int j = 0; j < 4; j++) p0[j] = __builtin_amdgcn_exp2f(sA[j]);
      f32x16 sB = mfma32(kn, (t & 1) ? qa : qf, f32x16{});
      if (!(t & 1)) kn = lds_b128((u < 4 ? cb + u * 1024 : cn) + ka);
#pragma unroll
      for (int j = 4; j < 8; j++) p0[j] = __builtin_amdgcn_exp2f(sA[j]);
      accs[t & 1] = pv_step(x0, p0, accs[t & 1]);
      if (t & 1) x0 = lds_b128((u < 4 ? cb + u * 1152 : cn) + va0);
#pragma unroll
      for (int j = 0; j < 8; j++) p1[j] = __builtin_amdgcn_exp2f(sA[8 + j]);
      accs[t & 1] = pv_step(x1, p1, accs[t & 1]);
      if (t & 1) x1 = lds_b128((u < 4 ? cb + u * 1152 : cn) + va1);
      sA = sB;
      __builtin_amdgcn_sched_group_barrier(0x400, 4, 0);
      __builtin_amdgcn_sched_group_barrier(0x008, 1, 0);
      if (!(t & 1)) __builtin_amdgcn_sched_group_barrier(0x100, 1, 0);
      __builtin_amdgcn_sched_group_barrier(0x400, 4, 0);
      __builtin_amdgcn_sched_group_barrier(0x008, 1, 0);
      if (t & 1) __builtin_amdgcn_sched_group_barrier(0x100, 1, 0);
      __builtin_amdgcn_sched_group_barrier(0x400, 8, 0);
      __builtin_amdgcn_sched_group_barrier(0x008, 1, 0);
      if (t & 1) __builtin_amdgcn_sched_group_barrier(0x100, 1, 0);
    }
    buf = nbuf;
  }
#undef AT_STAGE
  f32x16 acc;
  {
    char* ex = smem + AT_LDS;
    const f32x16 give = accs[0];
    *(LDSP(f32x4))(ex + w * 2304 + l * 16) = f32x4{give[0], give[1], give[2], give[3]};
    *(LDSP(f32x4))(ex + w * 2304 + 1024 + l * 16) = f32x4{give[4], give[5], give[6], give[7]};
    *(LDSP(float))(ex + w * 2304 + 2048 + l * 4) = give[8];
    BAR();
    const f32x4 r0 = *(const LDSP(f32x4))(ex + (w ^ 4) * 2304 + l * 16);
    const f32x4 r1 = *(const LDSP(f32x4))(ex + (w ^ 4) * 2304 + 1024 + l * 16);
    const float r2 = *(const LDSP(float))(ex + (w ^ 4) * 2304 + 2048 + l * 4);
    acc = accs[1];
#pragma unroll
    for (int j = 0; j < 4; j++) { acc[j] += r0[j]; acc[4 + j] += r1[j]; }
    acc[8] += r2;
  }
  float lsum = acc[8];
  bool ok = lsum >= 0x1p-40f && lsum <= 0x1p40f;
#pragma unroll
  for (int j = 0; j < 8; j++) ok = ok && (__builtin_fabsf(acc[j]) < 0x1p120f);
  if (__builtin_amdgcn_ballot_w64(!ok) != 0) {
    float m = -__builtin_inff();
    acc = f32x16{};
    const bf16x8 ones = __builtin_bit_cast(bf16x8, u32x4{0x3F803F80u, 0x3F803F80u, 0x3F803F80u, 0x3F803F80u});
    const char* vl = ws_c + WS_V + (size_t)head * 65536 + (ha * 16 + (d & 15)) * 16;
    for (int t = 0; t < 64; t++) {
      const bf16x8 kf = *(const bf16x8*)(kg + t * 1024 + l * 16);
      bf16x8 x0 = *(const bf16x8*)(vl + t * 1024), x1 = *(const bf16x8*)(vl + t * 1024 + 512);
      if (d >= 16) { x0 = ones; x1 = ones; }
      f32x16 s = mfma32(kf, qf, f32x16{});
      float tm = s[0];
#pragma unroll
      for (int j = 1; j < 16; j++) tm = __builtin_fmaxf(tm, s[j]);
      tm = __builtin_fmaxf(tm, __shfl_xor(tm, 32));
      const float mn = __builtin_fmaxf(m, tm);
      const float al = __builtin_amdgcn_exp2f(m - mn);
      m = mn;
      f32x8 p0, p1;
#pragma unroll
      for (int j = 0; j < 8; j++) { p0[j] = __builtin_amdgcn_exp2f(s[j] - mn); p1[j] = __builtin_amdgcn_exp2f(s[8 + j] - mn); }
#pragma unroll
      for (int j = 0; j < 16; j++) acc[j] *= al;
      acc = pv_step(x0, p0, acc);
      acc = pv_step(x1, p1, acc);
    }
    lsum = acc[8];
  }
  const float inv = 1.0f / lsum;
  u32x4 o;
#pragma unroll
  for (int j = 0; j < 4; j++) o[j] = pk_bf16(acc[2 * j] * inv, acc[2 * j + 1] * inv);
  st16(ws + WS_O + ((size_t)qt * 32 + head) * 1024, l * 16, o);
}

constexpr int GU_ST = 8192 + 16384;
constexpr int GU_LDS = 3 * GU_ST;

DEV void gateup_body(char* smem, const u16* __restrict__ w1, const u16* __restrict__ w3, char* ws, int mb, int nb) {
  const int tid = threadIdx.x, l = tid & 63;
  const int w = __builtin_amdgcn_readfirstlane(tid >> 6);
  const char* ag = ws + WS_XN + ((size_t)(mb * 4 + (w >> 1)) * 32 + (w & 1)) * 1024;
  const unsigned sm = lds_addr(smem);
  const u16* bg = ((w & 1) ? w3 : w1) + (size_t)((w >> 1) * 8) * F + nb * 128;
  const unsigned bvo = (((l & 31) >> 2) * F + (l >> 5) * 32 + (l & 3) * 8) * 2;
#define GU_STAGE(s, buf)                                                       \
  do {                                                                         \
    glds16(ag + (s) * 2048, l * 16, sm + (buf) * GU_ST + w * 1024);            \
    const u16* p_ = bg + (size_t)(s) * 32 * F;                                 \
    glds16(p_, bvo, sm + (buf) * GU_ST + 8192 + w * 2048);                     \
    glds16(p_ + 64, bvo, sm + (buf) * GU_ST + 8192 + w * 2048 + 1024);         \
  } while (0)
  GU_STAGE(0, 0);
  GU_STAGE(1, 1);
  const int mw = w & 1, nw = w >> 1, h = l >> 5;
  const int aoff = mw * 4096 + l * 16;
  const int boff = 8192 + h * 4096 + nw * 512 + ((l & 15) >> 2) * 64 + ((l >> 4) & 1) * 32 + (l & 3) * 8;
  f32x16 ga[2] = {}, ua[2] = {};
  int buf = 0;
  for (int s = 0; s < 16; s++) {
    if (s < 15) WAIT_V(3); else WAIT_V(0);
    BAR();
    if (s + 2 < 16) { const int nbuf = buf >= 1 ? buf - 1 : 2; GU_STAGE(s + 2, nbuf); }
    const char* sb = smem + buf * GU_ST;
#pragma unroll
    for (int ks = 0; ks < 2; ks++) {
      const bf16x8 a0 = lds_b128(sb + aoff + ks * 1024), a1 = lds_b128(sb + aoff + 2048 + ks * 1024);
      const char* bp = sb + boff + ks * 8192;
      const bf16x8 bg_ = lds_tr2(bp, bp + 256), bu_ = lds_tr2(bp + 2048, bp + 2304);
      ga[0] = mfma32(bg_, a0, ga[0]); ua[0] = mfma32(bu_, a0, ua[0]);
      ga[1] = mfma32(bg_, a1, ga[1]); ua[1] = mfma32(bu_, a1, ua[1]);
    }
    buf = buf == 2 ? 0 : buf + 1;
  }
#undef GU_STAGE
#pragma unroll
  for (int a = 0; a < 2; a++) {
    const int mblk = mb * 4 + mw * 2 + a;
#pragma unroll
    for (int hs = 0; hs < 2; hs++) {
      u32x4 o;
#pragma unroll
      for (int j = 0; j < 4; j++) {
        float r[2];
#pragma unroll
        for (int e = 0; e < 2; e++) {
          const float gv = ga[a][hs * 8 + 2 * j + e], uv = ua[a][hs * 8 + 2 * j + e];
          const float sg = __builtin_amdgcn_rcpf(1.0f + __builtin_amdgcn_exp2f(gv * -1.4426950408889634f));
          r[e] = gv * sg * uv;
        }
        o[j] = pk_bf16(r[0], r[1]);
      }
      const int ks = nb * 8 + nw * 2 + hs;
      st16(ws + WS_H + ((size_t)mblk * 112 + ks) * 1024, l * 16, o);
    }
  }
}

DEV void repack_body(char* smem, const u16* __restrict__ w2, const u16* __restrict__ wo, char* ws, int id) {
  const int l = threadIdx.x & 63;
  const int w = __builtin_amdgcn_readfirstlane(threadIdx.x >> 6);
  const unsigned svo = ((l >> 3) * D + ((l & 7) ^ (((l >> 4) & 1) << 2)) * 8) * 2;
  const unsigned ring = lds_addr(smem) + w * 8192;
  const int h = l >> 5, rr = (l & 15) >> 2;
  for (int t0 = 0; t0 < 5; t0 += 4) {
    const int n = t0 == 0 ? 4 : (id < 16 ? 1 : 0);
    for (int t = 0; t < n; t++) {
      const int s = id + 32 * (t0 + t);
      const u16* p = (s < 112 ? w2 + (size_t)s * 16 * D : wo + (size_t)(s - 112) * 16 * D) + w * 64;
      glds16(p, svo, ring + t * 2048);
      glds16(p + 8 * D, svo, ring + t * 2048 + 1024);
    }
    WAIT_V(0);
    for (int t = 0; t < n; t++) {
      const int s = id + 32 * (t0 + t);
#pragma unroll
      for (int nh = 0; nh < 2; nh++) {
        const char* bp = smem + w * 8192 + t * 2048 + (4 * h + rr) * 128 +
                         (((nh * 4 + ((l >> 4) & 1) * 2 + ((l & 3) >> 1)) ^ ((rr >> 1) << 2)) * 16) + (l & 1) * 8;
        st16(ws + WS_B, ((w * 144 + s) * 2 + nh) * 1024 + l * 16, __builtin_bit_cast(u32x4, lds_tr2(bp, bp + 1024)));
      }
    }
  }
}

constexpr int K2_LDS = GU_LDS > AT_LDS ? GU_LDS : AT_LDS;
static_assert(K2_LDS >= 8 * 8192, "repack ring");
static_assert(K2_LDS >= AT_LDS + 8 * 2304, "attention exchange area");
__global__ __launch_bounds__(512, 4) void k2_mid(const u16* __restrict__ w1, const u16* __restrict__ w3, char* __restrict__ ws,
                                                 const u16* __restrict__ w2, const u16* __restrict__ wo) {
  __shared__ __attribute__((aligned(16))) char smem[K2_LDS];
  const int b = blockIdx.x, x = b & 7, j = b >> 3, a = j & 31;
  if (j < 32) {
    __builtin_amdgcn_s_setprio(2);
    attn_body(smem, ws, ws, x * 4 + (a & 3), a >> 2);
  } else if (a < 28) {
    gateup_body(smem, w1, w3, ws, (x >> 1) * 4 + a / 7, (x & 1) * 7 + a % 7);
  } else {
    repack_body(smem, w2, wo, ws, x * 4 + (a - 28));
  }
}

constexpr int FN_D = 5;

DEV void gld16(bf16x8& dst, const void* base, unsigned voff) {
  asm volatile("global_load_dwordx4 %0, %1, %2" : "=&v"(dst) : "v"(voff), "s"(base) : "memory");
}
template <int N> DEV void wait_v4(bf16x8 (&f)[4]) {
  asm volatile("s_waitcnt vmcnt(%4)" : "+v"(f[0]), "+v"(f[1]), "+v"(f[2]), "+v"(f[3]) : "n"(N) : "memory");
}

__global__ __launch_bounds__(512, 2) void k3_final(const u16* __restrict__ x, char* __restrict__ ws, u16* __restrict__ out) {
  __shared__ __attribute__((aligned(16))) char smem[8 * 7 * 2048];
  const int l = threadIdx.x & 63;
  const int w = __builtin_amdgcn_readfirstlane(threadIdx.x >> 6);
  const int wa = w & 1, wn = (w >> 1) & 1, we = w >> 2;
  const int b = blockIdx.x, nb = (b >> 3) & 7, mb = (b & 7) * 4 + (b >> 6);
  const char* ah0 = ws + WS_H + ((size_t)(mb * 2 + wa) * 112 + w) * 1024;
  const char* ah1 = ws + WS_H + ((size_t)(mb * 2 + (wa ^ 1)) * 112 + w) * 1024;
  const char* ao0 = ws + WS_O + ((size_t)(mb * 2 + wa) * 32 + w) * 1024;
  const char* ao1 = ws + WS_O + ((size_t)(mb * 2 + (wa ^ 1)) * 32 + w) * 1024;
  const char* bf0 = ws + WS_B + ((size_t)(nb * 144 + w) * 2 + wn) * 1024;
  const char* bf1 = ws + WS_B + ((size_t)(nb * 144 + w) * 2 + (wn ^ 1)) * 1024;
  const unsigned va = l * 16, vb = (l ^ (we * 16)) * 16;
  bf16x8 f[FN_D][4];
#define FN_ISSUE(i, q)                                                         \
  gld16(f[(i) % FN_D][q], (q) == 0 ? ((i) < 14 ? ah0 + (i) * 8192 : ao0 + ((i) - 14) * 8192)   \
                        : (q) == 1 ? ((i) < 14 ? ah1 + (i) * 8192 : ao1 + ((i) - 14) * 8192)   \
                        : (q) == 2 ? bf0 + (i) * 16384 : bf1 + (i) * 16384, (q) < 2 ? va : vb)
  const int h = l >> 5;
  const unsigned go = ((mb * 64 + wa * 32 + (l & 31)) * D + nb * 64 + wn * 32 + we * 16 + 8 * h) * 2;
  bf16x8 xv;
  gld16(xv, x, go);
#pragma unroll
  for (int i = 0; i < FN_D - 1; i++)
#pragma unroll
    for (int q = 0; q < 4; q++) FN_ISSUE(i, q);
  f32x16 acc[4] = {};
#pragma unroll
  for (int i = 0; i < 18; i++) {
    bf16x8(&c)[4] = f[i % FN_D];
    switch (17 - i < FN_D - 2 ? 17 - i : FN_D - 2) {
      case 3: wait_v4<12>(c); break;
      case 2: wait_v4<8>(c); break;
      case 1: wait_v4<4>(c); break;
      default: wait_v4<0>(c); break;
    }
    acc[0] = mfma32(c[2], c[0], acc[0]);
    acc[1] = mfma32(c[2], c[1], acc[1]);
    acc[2] = mfma32(c[3], c[0], acc[2]);
    acc[3] = mfma32(c[3], c[1], acc[3]);
    SB();
    if (i + FN_D - 1 < 18) {
#pragma unroll
      for (int q = 0; q < 4; q++) FN_ISSUE(i + FN_D - 1, q);
    }
    SB();
  }
#undef FN_ISSUE
  static_assert(FN_D == 5, "wait counts");
#pragma unroll
  for (int c = 1; c < 8; c++)
#pragma unroll
    for (int j = 0; j < 2; j++) {
      const int o = (c >> 2) * 8 + j * 4;
      *(LDSP(f32x4))(smem + (((w ^ c) * 7 + c - 1) * 2 + j) * 1024 + l * 16) =
          f32x4{acc[c & 3][o], acc[c & 3][o + 1], acc[c & 3][o + 2], acc[c & 3][o + 3]};
    }
  BAR();
  asm volatile("" : "+v"(xv));
  f32x4 s[2];
#pragma unroll
  for (int j = 0; j < 2; j++) {
    s[j] = f32x4{acc[0][4 * j], acc[0][4 * j + 1], acc[0][4 * j + 2], acc[0][4 * j + 3]};
#pragma unroll
    for (int c = 1; c < 8; c++) s[j] += *(const LDSP(f32x4))(smem + ((w * 7 + c - 1) * 2 + j) * 1024 + l * 16);
  }
  float v[8];
#pragma unroll
  for (int q = 0; q < 4; q++) {
    auto t = __builtin_amdgcn_permlane32_swap(__builtin_bit_cast(unsigned, s[0][q] + 0.f), __builtin_bit_cast(unsigned, s[1][q] + 0.f), false, false);
    v[q] = __builtin_bit_cast(float, (unsigned)t[0]);
    v[4 + q] = __builtin_bit_cast(float, (unsigned)t[1]);
  }
  const u32x4 xr = __builtin_bit_cast(u32x4, xv);
  u32x4 o;
#pragma unroll
  for (int q = 0; q < 4; q++) o[q] = pk_bf16(v[2 * q] + bf_lo(xr[q]), v[2 * q + 1] + bf_hi(xr[q]));
  st16((char*)out, go, o);
}

extern "C" void kernel_launch(void* const* d_in, const int* in_sizes, int n_in, void* d_out, int out_size, void* d_ws,
                              size_t ws_size, hipStream_t stream) {
  const u16* x = (const u16*)d_in[0];
  const u16* wqkv = (const u16*)d_in[1];
  const u16* wo = (const u16*)d_in[2];
  const u16* w1 = (const u16*)d_in[4];
  const u16* w3 = (const u16*)d_in[5];
  const u16* w2 = (const u16*)d_in[6];
  char* ws = (char*)d_ws;
  k1_ln_qkv<<<256, 512, 0, stream>>>(x, wqkv, ws, w1, w3);
  k2_mid<<<512, 512, 0, stream>>>(w1, w3, ws, w2, wo);
  k3_final<<<256, 512, 0, stream>>>(x, ws, (u16*)d_out);
}
```
